```python
import torch

def _softmax(x, axis=-1):
    return torch.softmax(x, dim=axis)
def _gelu(x):
    return torch.nn.functional.gelu(x, approximate="tanh")
def _silu(x):
    return torch.nn.functional.silu(x)
def _sigmoid(x):
    return torch.sigmoid(x)
def _layernorm(x, axis=-1, eps=1e-5):
    m = x.mean(dim=axis, keepdim=True)
    v = x.var(dim=axis, keepdim=True, unbiased=False)
    return (x - m) / torch.sqrt(v + eps)
def _scatter_add(tgt, idx, src, axis=0):
    return tgt.clone().index_add_(axis, idx, src)
def _segment_sum(src, seg, n):
    out = torch.zeros((n,) + src.shape[1:], dtype=src.dtype)
    return out.index_add_(0, seg, src)
def _pad(x, pw):
    flat = tuple(v for pair in reversed(pw) for v in pair)
    return torch.nn.functional.pad(x, flat)

kernel_name = 'interleave_layers__ssm_conv_s4__sliding_local_dilated__prompt'

def setup_inputs():

    _w = (lambda a: (lambda t: t.to(torch.bfloat16) if t.is_floating_point() else t)(torch.as_tensor(a)))
    def _A_setup():
        B, L, D, N = 1, 2048, 512, 128
        A_re = -(lambda _x: torch.exp(torch.as_tensor(_x)))((lambda *s: torch.randn(*s, dtype=torch.bfloat16) * 1.6)(D, N))
        A_im = torch.pi * (lambda *s: torch.randn(*s, dtype=torch.bfloat16) * 0.1)(D, N)
        B_re = (lambda *s: torch.randn(*s, dtype=torch.bfloat16) * 0.1)(D, N)
        B_im = (lambda *s: torch.randn(*s, dtype=torch.bfloat16) * 0.1)(D, N)
        C_re = (lambda *s: torch.randn(*s, dtype=torch.bfloat16) * 0.1)(D, N)
        C_im = (lambda *s: torch.randn(*s, dtype=torch.bfloat16) * 0.1)(D, N)
        log_dt = (lambda *s: torch.randn(*s, dtype=torch.bfloat16) * 0.1)(D) * 0.5 - 4.0
        Dskip = (lambda *s: torch.randn(*s, dtype=torch.bfloat16) * 0.1)(D)
        return {
            "u": _w((lambda *s: torch.randn(*s, dtype=torch.bfloat16) * 0.05)(B, L, D)),
            "A_re": _w(A_re),
            "A_im": _w(A_im),
            "B_re": _w(B_re),
            "B_im": _w(B_im),
            "C_re": _w(C_re),
            "C_im": _w(C_im),
            "log_dt": _w(log_dt),
            "Dskip": _w(Dskip),
        }
    _A_in = _A_setup()

    def _B_setup():
        B, S, D = 1, 2048, 512
        return {
            "x": _w((lambda *s: torch.randn(*s, dtype=torch.bfloat16) * 0.05)(B, S, D)),
            "wqkv": _w((lambda *s: torch.randn(*s, dtype=torch.bfloat16) * 0.05)(D, 3 * D)),
            "wo": _w((lambda *s: torch.randn(*s, dtype=torch.bfloat16) * 0.05)(D, D)),
        }
    _B_in = _B_setup()

    return {
        "A_u": _A_in["u"],
        "A_A_re": _A_in["A_re"],
        "A_A_im": _A_in["A_im"],
        "A_B_re": _A_in["B_re"],
        "A_B_im": _A_in["B_im"],
        "A_C_re": _A_in["C_re"],
        "A_C_im": _A_in["C_im"],
        "A_log_dt": _A_in["log_dt"],
        "A_Dskip": _A_in["Dskip"],
        "B_x": _B_in["x"],
        "B_wqkv": _B_in["wqkv"],
        "B_wo": _B_in["wo"],
    }

def reference(A_u, A_A_re, A_A_im, A_B_re, A_B_im, A_C_re, A_C_im, A_log_dt, A_Dskip, B_x, B_wqkv, B_wo):
    def _A_ref(u, A_re, A_im, B_re, B_im, C_re, C_im, log_dt, Dskip):
        Bsz, L, D = u.shape
        dt = torch.exp(log_dt)[:, None]
        A = A_re + 1j * A_im
        Bc = B_re + 1j * B_im
        Cc = C_re + 1j * C_im
        logdA = dt * A
        dB = dt * Bc
        l = (lambda x, dt: x.to(dt))(torch.arange(L), A_re.dtype)
        P = torch.exp(logdA[:, :, None] * l[None, None, :])
        K = torch.einsum("dn,dnl,dn->dl", Cc, P, dB).real
        L2 = 2 * L
        Kp = _pad(K, ((0, 0), (0, L)))
        up = (lambda x, *d: x.permute(*d))(u, 0, 2, 1)
        up = _pad(up, ((0, 0), (0, 0), (0, L)))
        Kf = (lambda x, axes=(-2,-1): torch.fft.rfft2(x.float(), dim=axes))(Kp[:, None, :], axes=(-2, -1))
        Uf = (lambda x, axes=(-2,-1): torch.fft.rfft2(x.float(), dim=axes))(up[:, :, None, :], axes=(-2, -1))
        Yf = Uf * Kf[None, :, :, :]
        y = (lambda x, s, axes=(-2,-1): torch.fft.irfft2(x, s=s, dim=axes).to(torch.bfloat16))(Yf, (1, L2), axes=(-2, -1))[:, :, 0, :L]
        y = (lambda x, *d: x.permute(*d))(y, 0, 2, 1) + u * Dskip[None, None, :]
        return y

    def _B_ref(x, wqkv, wo):
        B, S, D = x.shape
        H = 32; Dh = D // H
        W, dil = 8, 2
        qkv = (lambda x, *d: x.permute(*d))((x @ wqkv).reshape(B, S, 3, H, Dh), 2, 0, 3, 1, 4)
        q, k, v = qkv[0], qkv[1], qkv[2]
        scale = Dh ** -0.5
        pos = torch.arange(S)
        off = torch.arange(W) * dil
        idx = pos[:, None] - off[None, :]
        mask = idx >= 0
        idx_c = torch.where(mask, idx, 0).reshape(-1)
        kg = k[:, :, idx_c, :].reshape(B, H, S, W, Dh)
        vg = v[:, :, idx_c, :].reshape(B, H, S, W, Dh)
        logits = torch.einsum("bhsd,bhswd->bhsw", q, kg) * scale
        logits = torch.where(mask[None, None, :, :], logits, -1e9)
        attn = _softmax(logits, axis=-1)
        o = torch.einsum("bhsw,bhswd->bhsd", attn, vg)
        return (lambda x, *d: x.permute(*d))(o, 0, 2, 1, 3).reshape(B, S, D) @ wo

    x = A_u
    x = x + _A_ref(_layernorm(x, axis=-1), A_A_re, A_A_im, A_B_re, A_B_im, A_C_re, A_C_im, A_log_dt, A_Dskip)
    x = x + _B_ref(_layernorm(x, axis=-1), B_wqkv, B_wo)
    x = x + _A_ref(_layernorm(x, axis=-1), A_A_re, A_A_im, A_B_re, A_B_im, A_C_re, A_C_im, A_log_dt, A_Dskip)
    x = x + _B_ref(_layernorm(x, axis=-1), B_wqkv, B_wo)
    return x
```

```cpp
#if defined(__HIP_DEVICE_COMPILE__)
#pragma clang attribute push (__attribute__((target("no-packed-fp32-ops"))), apply_to = function)
#endif
#include <hip/hip_runtime.h>
#include <hip/hip_bf16.h>
#include <stdint.h>
#define TIDX ((int)__builtin_amdgcn_workitem_id_x())
#define BIDX ((int)__builtin_amdgcn_workgroup_id_x())
#define ST(set, k)
#define ST_INIT()
#define ST_OUT()
#define KATTR
#define FAILC(k)
#define DROLE_ 0

typedef unsigned short u16;
typedef unsigned int u32;
typedef unsigned long long u64;
using bf16x8 = __attribute__((ext_vector_type(8))) short;
using bf16x4 = __attribute__((ext_vector_type(4))) short;
using f16x8 = __attribute__((ext_vector_type(8))) _Float16;
using f16x4 = __attribute__((ext_vector_type(4))) _Float16;
using f32x2 = __attribute__((ext_vector_type(2))) float;
using f32x4 = __attribute__((ext_vector_type(4))) float;
using u32x2 = __attribute__((ext_vector_type(2))) unsigned;
using u32x4 = __attribute__((ext_vector_type(4))) unsigned;
#define LAS __attribute__((address_space(3)))
#define GAS __attribute__((address_space(1)))

#define D_ 512
#define NS_ 128
#define XS_ 2080

#define MB_ (1 << 20)
#define XTO(kp_) (OFF_O1 + (((kp_) >> 4) & 1) * MB_ + ((kp_) >> 5) * 131072 + ((kp_) & 15) * 8192)
#define XTS 8192
#define OFF_ST0 (5 * MB_)
#define OFF_SG (OFF_ST0 + 32768)
#define OFF_ST2 (OFF_ST0 + 65536)
#define OFF_EP (OFF_ST0 + 131072)
#define OFF_WQ (9 * MB_ - 262144)
#define OFF_WO (10 * MB_ + 393216)
#define OFF_X1 (11 * MB_)
#define OFF_O1 (14 * MB_)
#define OFF_X2 (16 * MB_)
#define OFF_X3 (19 * MB_)
#define OFF_O2 (22 * MB_)
#define OFF_KV1 (24 * MB_)
#define OFF_KV2 (28 * MB_)
#define KVL 0
#define KVO(rb_, ly_, h_, hd_) (OFF_KV1 + (KVL == 0 ? (((ly_) * 2 + (h_)) * 1024 + (rb_) * 32 + (hd_)) * 2048 : KVL == 1 ? ((rb_) * 128 + ((ly_) * 2 + (h_)) * 32 + (hd_)) * 2048 : (((rb_) * 32 + (hd_)) * 4 + (ly_) * 2 + (h_)) * 2048))
#define WSB (32 * MB_)
#define XBS 16768
#define SPIN_CAP (1u << 17)
#define SLP_U 2
#define SLP_F 2
#define SLP_G 1
#define R1D 3
#define R1E 4
#define FLS 1
#define HA 13
#define WOE 4
#define CSA 3
#define SLP_H1 0
#define SLP_H2 2
#define SLP_FL 2
#define OSA 17
#define OP2 5
#define KV1 51
#define KV2 51
#define KW1 76
#define KW2 76
#define KWN (LY ? KW2 : KW1)
#define PAGE(k_) do { asm volatile("s_branch 3f\n\t.p2align 12\n3:"); } while (0)
#define PAGEV(k_) do { asm volatile("s_branch 3f\n\t.p2align 12\n.Lpg%0:\n\ts_branch .Lbk%0\n3:" :: "n"(k_)); } while (0)
#define PAGEW(k_) do { asm volatile("s_branch 3f\n.Lpg%0:\n\ts_branch 4f\n\t.p2align 12\n4:\n\ts_branch .Lbk%0\n3:" :: "n"(k_)); } while (0)
#define VISIT(k_) asm volatile("s_branch .Lpg%0\n.Lbk%0:" :: "n"(k_))
#define GXH 3
#define GL1 2
#define SLP_XR1 0
#define SLP_XR2 0
#define SLP_XR3 0
#define SLP_XR4 0
#define SLP_OR 0
#define FK 0
#define SIG 1
#define NSG 0
#define XEG 1
#define GPE 37
#define PA1 99
#define PA2 99
#define PB1 99
#define PB2 99
#define GPR 0
#define DBL 0
#define EX1 0
#define EX2 1
#define PFK 0
#define SLP_G1 0
#define SLP_G2 0
#define SLP_HG 1
#define NH1 96
#define NH2 93
#define SLP_HR 1
#define WO1 8
#define SLP_XA2 4
#define GPF 4
#define WSP 3
#define PFS2 22
#define PFD 3
#define SLP_XP 2
#define SLP_SP 4
#define SLP_S 2
#define SLP_XA1 0
#define SLP_XB1 5
#define SLP_XB2 8
#define SLP_OB1 4
#define SLP_OB2 9
#define SLP_OA1 8
#define SLP_OA2 8
#define SLP_C2 5
#define SLP_C3 10

__device__ __forceinline__ float bf2f(u16 v) { return __uint_as_float(((u32)v) << 16); }
typedef __attribute__((ext_vector_type(2))) __bf16 bf16x2v;
__device__ __forceinline__ u32 pk2(float a, float b) {
  f32x2 f = {a, b};
  bf16x2v v = __builtin_convertvector(f, bf16x2v);
  return __builtin_bit_cast(u32, v);
}
__device__ __forceinline__ float rbf(float f) { return __uint_as_float(pk2(f, 0.f) << 16); }
__device__ __forceinline__ float lo2f(u32 w) { return __uint_as_float(w << 16); }
__device__ __forceinline__ float hi2f(u32 w) { return __uint_as_float(w & 0xFFFF0000u); }
template <int CTRL>
__device__ __forceinline__ float dpp_mov(float v) {
  return __int_as_float(__builtin_amdgcn_update_dpp(0, __float_as_int(v), CTRL, 0xf, 0xf, false));
}
#define DPP_XOR1 0xB1
#define DPP_XOR2 0x4E
#define DPP_ROR8 0x128
template <int CTRL, int RMASK>
__device__ __forceinline__ float dpp_add(float v) {
  return v + __int_as_float(__builtin_amdgcn_update_dpp(0, __float_as_int(v), CTRL, RMASK, 0xf, false));
}
__device__ __forceinline__ float rmax(float a, float b) {
  float r;
  asm("v_max_f32 %0, %1, %2" : "=v"(r) : "v"(a), "v"(b));
  return r;
}
__device__ __forceinline__ void lds_barrier() {
  asm volatile("s_waitcnt lgkmcnt(0)" ::: "memory");
  __builtin_amdgcn_s_barrier();
  asm volatile("" ::: "memory");
}
__device__ __forceinline__ void cexp2(float re2, float rev, float& orr, float& oi) {
  float e = __builtin_amdgcn_exp2f(re2);
  rev = __builtin_amdgcn_fractf(rev);
  float s = __builtin_amdgcn_sinf(rev);
  float c = __builtin_amdgcn_cosf(rev);
  orr = e * c;
  oi = e * s;
}
__device__ __forceinline__ float dot2acc(u32 a, u32 b, float c) {
  return __builtin_amdgcn_fdot2_f32_bf16(__builtin_bit_cast(bf16x2v, a), __builtin_bit_cast(bf16x2v, b), c, false);
}
__device__ __forceinline__ u32 opaque(u32 v) { asm volatile("" : "+v"(v)); return v; }
__device__ __forceinline__ int lane_id() { return (int)__builtin_amdgcn_mbcnt_hi(~0u, __builtin_amdgcn_mbcnt_lo(~0u, opaque(0u))); }
__device__ __forceinline__ u32 pack_stats(float mean, float var) {
  float vb = rbf(var);
  float ve = rbf(vb + 1e-5f);
  float sd = __builtin_amdgcn_sqrtf(ve);
  return pk2(mean, sd);
}

#define SB() __builtin_amdgcn_sched_barrier(0)
template <int V> struct IC { static constexpr int value = V; };
template <int B, int E, class F>
__device__ __forceinline__ void static_for(F& f) { if constexpr (B < E) { f(IC<B>{}); static_for<B + 1, E>(f); } }
typedef __amdgpu_buffer_rsrc_t rsrc_t;
__device__ __forceinline__ u32x4 ldw(rsrc_t r, int off) { return __builtin_amdgcn_raw_buffer_load_b128(r, off, 0, 16); }
__device__ __forceinline__ u32x4 ldws(rsrc_t r, int off, int soff) { return __builtin_amdgcn_raw_buffer_load_b128(r, off, soff, 16); }
__device__ __forceinline__ void stw(rsrc_t r, int off, u32x4 v) { __builtin_amdgcn_raw_buffer_store_b128(v, r, off, 0, 16); }
__device__ __forceinline__ u32 umax4(u32 m, u32x4 v) { return max(max(max(m, v[0]), v[1]), max(v[2], v[3])); }
template <int CTRL>
__device__ __forceinline__ u32 dpp_u(u32 v) { return (u32)__builtin_amdgcn_update_dpp(0, (int)v, CTRL, 0xf, 0xf, false); }

#define CSTR 4160
#define L_U 33280
#define L_TA 42496
#define L_TB (L_TA + 32768)
#define L_QB (L_TB + 32768)
#define L_TH (L_QB + 16384)
#define L_SP (L_QB + 32768)
#define L_FS (L_SP + 4096)
#define L_MI (L_FS + 3072)
#define L_XS (L_MI + 80)
#define L_WX (L_XS + 1024)
#define L_BD (L_WX + 12288)
#define MEGA_LDS (L_BD + 2048)
#define L_O2 8192
#define L_REDA L_QB
#define L_REDB (L_QB + 16384)
#define L_XC L_REDB
#define L_X (L_REDB + 8192)
#define TBS 3136
#define L_WR (L_QB + 24576)
#define L_WS L_TA
#define L_KT L_TA
#define FTS 528
#define L_P0 (L_FS + 2048)
#define C_OB 20
#define C_HR 16
#define C_OT 24
#define C_X2 28
#define C_G2 32
#define C_A 36
#define C_B 40
#define C_F 44
#define C_KH 48
#define C_P2 52
#define C_XA 8
#define C_P3 64
#define C_U 56
#define C_S 72
#define C_W 60

typedef __attribute__((ext_vector_type(2))) _Float16 f16x2v;
typedef __attribute__((ext_vector_type(2))) short i16x2;
__device__ __forceinline__ u32 pkh(float a, float b) {
  u32 r;
  asm("v_cvt_pk_f16_f32 %0, %1, %2" : "=v"(r) : "v"(a), "v"(b));
  return r;
}
__device__ __forceinline__ u32 pkhn(float a, float b) {
  u32 r;
  asm("v_cvt_pk_f16_f32 %0, %1, -%2" : "=v"(r) : "v"(a), "v"(b));
  return r;
}
__device__ __forceinline__ u32 tb_addr(u32 bits2, u32 base2) {
  i16x2 b = __builtin_elementwise_max(__builtin_bit_cast(i16x2, bits2), (i16x2){0x3F00, 0x3F00});
  u32 r;
  asm("v_pk_mad_u16 %0, %1, 2, %2 op_sel_hi:[1,0,1]" : "=v"(r) : "v"(b), "s"(base2));
  return r;
}
__device__ __forceinline__ int bexp(float b) { return min(max((int)((__float_as_uint(b) >> 23) & 0xFF), 20), 240); }
__device__ __forceinline__ void cnt_add(char* smem, int off, int lane) {
  ST(2, 2);
  u32 t;
  asm volatile("s_mov_b64 exec, 1\n\tv_mov_b32 %0, %1\n\tds_inc_u32 %0, %0\n\ts_mov_b64 exec, -1" : "=v"(t) : "is"(L_MI + off) : "memory");
}
template <int SL = 2>
__device__ __forceinline__ void cnt_wait(char* smem, int off, u32 target) {
#pragma unroll 1
  for (unsigned n = 0; n < (1u << 23) && (u32)__builtin_amdgcn_readfirstlane(__hip_atomic_load((LAS u32*)(smem + L_MI + off), __ATOMIC_RELAXED, __HIP_MEMORY_SCOPE_WORKGROUP)) < target; ++n)
    __builtin_amdgcn_s_sleep(SL);
  asm volatile("" ::: "memory");
}
__device__ __forceinline__ void cnt_spin(char* smem, int off, u32 target) {
  u32 a, v, c, n;
  asm volatile("v_mov_b32 %0, %4\n\ts_brev_b32 %3, 64\n0:\n\tds_read_b32 %1, %0\n\ts_sub_u32 %3, %3, 1\n\ts_cbranch_scc1 1f\n\ts_waitcnt lgkmcnt(0)\n\tv_readfirstlane_b32 %2, %1\n\ts_cmp_lt_u32 %2, %5\n\ts_cbranch_scc1 0b\n1:"
               : "=&v"(a), "=&v"(v), "=&s"(c), "=&s"(n) : "is"(L_MI + off), "is"(target) : "scc", "memory");
  __builtin_amdgcn_s_waitcnt(0xC07F);
  ST(2, 3);
}
__device__ __forceinline__ u32 x0_mark(u32 tag) { return 0xFF807F80u + (((tag & 31u) + 1u) << 16) + ((tag >> 5) & 31u) + 1u; }
#define NE2(a_, b_, t_) __builtin_amdgcn_bitop3_b32(a_, b_, t_, 0x7e)
__device__ __forceinline__ bool tags_ok(const u32x4& f, u32 tag) { return !__any((NE2(f[0], f[1], tag) | NE2(f[2], f[3], tag)) != 0u); }
template <bool RARE = false>
__device__ __forceinline__ void tags_wait(u32x4& f, rsrc_t wrs, int fo, u32 tag, int fid) {
  if (RARE ? __builtin_expect(!tags_ok(f, tag), 0) : !tags_ok(f, tag)) {
    for (unsigned spins = 0; spins <= SPIN_CAP; ++spins) {
      FAILC(fid);
      __builtin_amdgcn_s_sleep(SLP_F);
      asm volatile("" ::: "memory");
      if (tags_ok(ldw(wrs, fo), tag)) break;
    }
  }
  asm volatile("" ::: "memory");
}
__device__ __forceinline__ void put_copies(u32 cop, u32x4 c0, u32x4 c1, u32x4 c2, u32x4 c3) {
  asm volatile("" : "+v"(c0), "+v"(c1), "+v"(c2), "+v"(c3));
  LAS u32x4* q = (LAS u32x4*)(size_t)cop;
  q[0] = c0; q[CSTR / 16] = c1; q[2 * CSTR / 16] = c2; q[3 * CSTR / 16] = c3;
  asm volatile("s_nop 1" :: "v"(c0), "v"(c1), "v"(c2), "v"(c3) : "memory");
}
struct ConvPre { f32x4 x0, x1; };
__device__ __forceinline__ void conv_ln(char* smem, int l0, ConvPre& P, f32x4 rs, f32x4 nm) {
  u32x2 u0 = {pkh(fmaf(P.x0[0], rs[0], nm[0]), fmaf(P.x0[1], rs[1], nm[1])), pkh(fmaf(P.x0[2], rs[2], nm[2]), fmaf(P.x0[3], rs[3], nm[3]))};
  u32x2 u1 = {pkh(fmaf(P.x1[0], rs[0], nm[0]), fmaf(P.x1[1], rs[1], nm[1])), pkh(fmaf(P.x1[2], rs[2], nm[2]), fmaf(P.x1[3], rs[3], nm[3]))};
  asm volatile("" : "+v"(u0), "+v"(u1));
  *(u32x2*)(smem + L_U + (256 + l0) * 2) = u0;
  *(u32x2*)(smem + L_U + 4608 + (256 + l0) * 2) = u1;
  asm volatile("s_nop 1" :: "v"(u0), "v"(u1) : "memory");
}
__device__ __forceinline__ void conv_unp(ConvPre& P, u32x4 xw) {
#pragma unroll
  for (int e = 0; e < 4; ++e) { P.x0[e] = lo2f(xw[e]); P.x1[e] = hi2f(xw[e]); }
  asm volatile("" : "+v"(P.x0), "+v"(P.x1));
}
__device__ __forceinline__ u32x4 x2_ld(rsrc_t wrs, int kp, int l0) { return ldw(wrs, OFF_X2 + (kp * XS_ + l0) * 4); }
__device__ __forceinline__ void x2_poll(rsrc_t wrs, int kp, int l0, u32x4& xw) {
#pragma unroll 1
  for (int spins = SPIN_CAP;; --spins) {
    asm volatile("" ::: "memory");
    xw = x2_ld(wrs, kp, l0);
    if (!__any(umax4(0u, xw) == 0xFFFFFFFFu && spins > 0)) break;
    FAILC(16 + (__builtin_amdgcn_readfirstlane(l0) >> 10));
    __builtin_amdgcn_s_sleep(SLP_XP);
  }
}
__device__ __forceinline__ void st2_ld(rsrc_t wrs, int l0, u32x4 (&g)[4]) {
#pragma unroll
  for (int e = 0; e < 4; ++e) g[e] = ldw(wrs, OFF_ST2 + ((l0 & ~255) + ((l0 >> 2) & 63)) * 16 + e * 1024);
}
__device__ __forceinline__ u32 st2_and(const u32x4 (&g)[4]) {
  u32 ok = 32u;
#pragma unroll
  for (int e = 0; e < 4; ++e) ok &= g[e][0] & g[e][2];
  return ok;
}
__device__ __forceinline__ void st2_poll(rsrc_t wrs, int l0, u32x4 (&g)[4]) {
#pragma unroll 1
  for (int spins = SPIN_CAP;; --spins) {
    asm volatile("" ::: "memory");
    st2_ld(wrs, l0, g);
    if (!__any(st2_and(g) == 0u && spins > 0)) break;
    FAILC(7 + (__builtin_amdgcn_readfirstlane(l0) >> 10));
    __builtin_amdgcn_s_sleep(SLP_SP);
  }
}
__device__ __forceinline__ void conv2_in(char* smem, int l0, ConvPre& P, u32x4 (&g)[4]) {
  f32x4 rs, nm;
#pragma unroll
  for (int e = 0; e < 4; ++e) {
    const float c1 = 1.0f / 512.0f, c2 = 1.0f / (512.0f * 4294967296.0f);
    const float m1 = fmaf((float)(int)g[e][1], c1, (float)g[e][0] * c2);
    const float qq = fmaf((float)(int)g[e][3], c1, fmaf((float)g[e][2], c2, 1e-5f));
    rs[e] = __builtin_amdgcn_rsqf(rmax(fmaf(-m1, m1, qq), 1e-5f));
    nm[e] = -m1 * rs[e];
  }
  conv_ln(smem, l0, P, rs, nm);
}
__device__ __forceinline__ void conv_xch(ConvPre& P) {
  const bool c = __builtin_amdgcn_inverse_ballot_w64(0xAAAAAAAAAAAAAAAAull);
  asm volatile("" : "+v"(P.x0), "+v"(P.x1));
#pragma unroll
  for (int e = 0; e < 4; ++e) {
    const float r = __int_as_float(__builtin_amdgcn_mov_dpp(__float_as_int(c ? P.x0[e] : P.x1[e]), DPP_XOR1, 0xf, 0xf, true));
    P.x0[e] = c ? r : P.x0[e]; P.x1[e] = c ? P.x1[e] : r;
  }
  asm volatile("" : "+v"(P.x0), "+v"(P.x1));
}
template <int O>
__device__ __forceinline__ void conv_rd(f32x4 (&q)[4], u32 r0, u32 r1) {
  const LAS f32x4 *p0 = (const LAS f32x4*)(size_t)r0 + O, *p1 = (const LAS f32x4*)(size_t)r1 + O;
  q[0] = p0[0]; q[1] = p0[256]; q[2] = p1[0]; q[3] = p1[256];
  __builtin_amdgcn_sched_barrier(0);
}
__device__ __forceinline__ u32x4 conv_epi(const f32x4 (&q)[4], float isc, const ConvPre& P) {
  const f32x4 a0 = q[0], a1 = q[1], b0 = q[2], b1 = q[3];
  u32x4 v;
#pragma unroll
  for (int e = 0; e < 2; ++e) {
    v[e] = pk2(fmaf(a1[2 * e], isc, fmaf(a0[2 * e], isc, P.x0[2 * e])), fmaf(a1[2 * e + 1], isc, fmaf(a0[2 * e + 1], isc, P.x0[2 * e + 1])));
    v[2 + e] = pk2(fmaf(b1[2 * e], isc, fmaf(b0[2 * e], isc, P.x1[2 * e])), fmaf(b1[2 * e + 1], isc, fmaf(b0[2 * e + 1], isc, P.x1[2 * e + 1])));
  }
  return v;
}
__device__ __forceinline__ void dma4k(rsrc_t r, int voff, int soff, int lds) {
  asm volatile("s_mov_b32 m0, %3\n\ts_nop 0\n\t"
               "buffer_load_dwordx4 %0, %1, %2 offen sc1 lds\n\t"
               "buffer_load_dwordx4 %0, %1, %2 offen offset:1024 sc1 lds\n\t"
               "buffer_load_dwordx4 %0, %1, %2 offen offset:2048 sc1 lds\n\t"
               "buffer_load_dwordx4 %0, %1, %2 offen offset:3072 sc1 lds"
               :: "v"(voff), "s"(r), "s"(soff), "s"(lds) : "memory");
}
__device__ __forceinline__ void x_dma(rsrc_t wrs, int xin, int rb16, int tile, int a, int lane) {
  const int vo = lane * 16;
  if (xin == OFF_X1) {
    if (tile == L_TB && !EX1) dma4k(wrs, ((lane >> 1) & 1) * (XBS - 32) + (lane >> 2) * 64 + 32 + (lane & 1) * 16, xin + (rb16 - 2) * XBS + a * 4096, L_TH + a * 4096);
#pragma unroll
    for (int e = 0; e < 4; ++e)
      asm volatile("s_mov_b32 m0, %3\n\ts_nop 0\n\tbuffer_load_dwordx4 %0, %1, %2 offen offset:%4 sc1 lds\n\tbuffer_load_dwordx4 %0, %1, %2 offen offset:%5 sc1 lds"
                   :: "v"(vo), "s"(wrs), "s"(xin + (rb16 + (e & 1)) * XBS + a * 4096), "s"(tile + (e & 1) * 16384 + a * 4096), "n"((e >> 1) * 2048), "n"((e >> 1) * 2048 + 1024) : "memory");
    return;
  }
  if (tile != L_TB || EX2) dma4k(wrs, vo, xin + (rb16 + 1) * XBS + a * 4096, tile + 16384 + a * 4096);
  dma4k(wrs, vo, xin + rb16 * XBS + a * 4096, tile + a * 4096);
  if (tile == L_TB && !EX2) {
    dma4k(wrs, vo, xin + (rb16 - 1) * XBS + a * 4096, L_TH + a * 4096);
    dma4k(wrs, vo, xin + (rb16 + 1) * XBS + a * 4096, tile + 16384 + a * 4096);
  }
}
__device__ __forceinline__ bf16x8 ld_tr8(const LAS char* p) {
  const bf16x4 lo = __builtin_amdgcn_ds_read_tr16_b64_v4i16((LAS bf16x4*)p), hi = __builtin_amdgcn_ds_read_tr16_b64_v4i16((LAS bf16x4*)(p + 512));
  return __builtin_shufflevector(lo, hi, 0, 1, 2, 3, 4, 5, 6, 7);
}

__device__ __forceinline__ void prefill_o2(rsrc_t wrs, int a, int lane, int e) {
  stw(wrs, OFF_O2 + a * 524288 + BIDX * 2048 + e * 1024 + lane * 16, (u32x4){0xFFFFFFFFu, 0xFFFFFFFFu, 0xFFFFFFFFu, 0xFFFFFFFFu});
}
struct Far { f16x8 u[2][2], bd[2], t[4], k4; f32x4 d[2][2]; u32x4 v[2]; };
#define FMA_(z_, d_, a_, b_) do { __builtin_amdgcn_sched_barrier(0); if (z_) asm volatile("v_mfma_f32_16x16x32_f16 %0, %1, %2, 0" : "=&v"(d_) : "v"(a_), "v"(b_)); \
      else asm volatile("v_mfma_f32_16x16x32_f16 %0, %1, %2, %0" : "+v"(d_) : "v"(a_), "v"(b_)); __builtin_amdgcn_sched_barrier(0); } while (0)
#define FKB(Q_, h_) ((h_) ? 3 - (Q_) : (Q_))
#define F_LU(F_, Q_, e_, ub_) F_.u[(e_) >> 1][(e_) & 1] = *(const LAS f16x8*)((ub_) + 512 * FKB(Q_, (e_) >> 1) - 16 + 16 * ((e_) & 1))
#define F_WS(F_, e_) FMA_(true, F_.d[(e_) >> 1][(e_) & 1], F_.u[(e_) >> 1][(e_) & 1], F_.bd[(e_) & 1])
#define F_CV(F_, e_) asm volatile("v_cvt_pk_f16_f32 %0, %1, %2" : "=v"(F_.v[(e_) >> 2][(e_) & 3]) : "v"(F_.d[(e_) >> 2][0][(e_) & 3]), "v"(F_.d[(e_) >> 2][1][(e_) & 3]))
#define F_MM(F_, Q_, e_, acc_) do { \
      if ((e_) < 4 - (Q_)) FMA_((e_) == 0, acc_[(3 - (e_)) & 3], F_.v[0], F_.t[(3 - (e_) - (Q_)) & 3]); \
      else if ((e_) < 5) FMA_(false, acc_[((e_) - 1) & 3], F_.v[1], F_.t[((e_) - 4 + (Q_)) & 3]); \
      else FMA_(false, acc_[FKB(Q_, (e_) == 6)], F_.k4, F_.u[(e_) == 6][1]); } while (0)
struct CPre { u32 ah, ah2, bp, ra; f16x8 fh[4][4]; };
#define LDA(d_, o_) do { f16x4 h0_ = *(const LAS f16x4*)((const LAS char*)(size_t)C.ah + 3776 - (o_)), h1_ = *(const LAS f16x4*)((const LAS char*)(size_t)C.ah2 + 3776 - (o_)); \
      d_ = __builtin_shufflevector(h0_, h1_, 0, 1, 2, 3, 4, 5, 6, 7); } while (0)
__device__ __forceinline__ void c_pre(CPre& C, int a, int lane) {
  lane = (int)opaque((u32)lane);
  const int c = a >> 1, qq = a & 1, j16 = lane & 15, kg = lane >> 4;
  const int y00 = 2031 - j16 + 8 * kg - 128 * qq;
  const int sg = y00 & 3;
  C.ah = opaque((u32)(c * 4 * CSTR + sg * CSTR + 2 * (y00 - sg) - 3776));
  C.ah2 = opaque(C.ah + 8);
  C.bp = opaque((u32)(L_U + c * 4608 + (256 + 16 * j16 - 16 + 8 * kg - 128 * qq) * 2 - 192));
  C.ra = opaque((u32)(L_REDA + (a * 256 + 4 * j16 + (kg ^ ((j16 >> 1) & 3) ^ c)) * 16));
#pragma unroll
  for (int e = 0; e < 16; ++e) { LDA(C.fh[e >> 2][e & 3], 512 * (e & 3) + 64 * (e >> 2)); __builtin_amdgcn_sched_barrier(0); }
#pragma unroll
  for (int e = 0; e < 16; ++e) asm volatile("" : "+v"(C.fh[e >> 2][e & 3]));
}
__device__ __forceinline__ void a_conv(char* smem, rsrc_t wrs, CPre& C, int a, int lane) {
  LAS f32x4* ra = (LAS f32x4*)(size_t)C.ra;
  f32x4 acc[8];
  f16x8 bf[4][8];
#define CSB() __builtin_amdgcn_sched_barrier(0)
#define LDB(r_, t_) bf[r_][t_] = *(const LAS f16x8*)((const LAS char*)(size_t)C.bp + 512 * (t_) + 192 - 64 * (r_))
#define CMF(z_, cb_, r_, mu_) do { CSB(); if (z_) asm volatile("v_mfma_f32_16x16x32_f16 %0, %1, %2, 0" : "=&v"(acc[cb_]) : "v"(C.fh[r_][mu_]), "v"(bf[r_][(cb_) - (mu_)])); \
      else asm volatile("v_mfma_f32_16x16x32_f16 %0, %1, %2, %0" : "+v"(acc[cb_]) : "v"(C.fh[r_][mu_]), "v"(bf[r_][(cb_) - (mu_)])); CSB(); } while (0)
  auto tri = [&](int T) __attribute__((always_inline)) {
#pragma unroll
    for (int e = 0; e < 8; ++e) { LDB(e < 4 ? 0 : (e - 4) % 3 + 1, T + (e < 4 ? e : (e - 4) / 3)); CSB(); }
    int i = 0;
#pragma unroll
    for (int mu = 0; mu < 4; ++mu)
#pragma unroll
      for (int cb = mu; cb < 4; ++cb, ++i) { CMF(!T && !mu, T + cb, 0, mu); if (i < 8) LDB((i + 4) % 3 + 1, T + (i + 4) / 3); }
#pragma unroll
    for (int cb = 0; cb < 4; ++cb)
#pragma unroll
      for (int e = 0; e < 3 * cb + 3; ++e) {
        CMF(false, T + cb, e % 3 + 1, cb - e / 3);
        if (cb && e == 1) ra[256 * T + (cb - 1) * 64] = acc[T + cb - 1];
      }
  };
  asm volatile("s_barrier" ::: "memory");
  ST(1, 7);
  tri(0);
  Far F;
  u32 tp, bq, ub[2];
#pragma unroll
  for (int n = 0; n < 28; ++n) {
    const int nd = n < 16 ? n : n - 4, rr = nd / 6, k = nd % 6, mu = k < 1 ? 1 : k < 3 ? 2 : 3, cb = 4 + k - mu * (mu - 1) / 2;
    if (n >= 16 && n < 20) F_WS(F, n & 3); else CMF(rr == 0 && cb - mu == 3, cb, rr, mu);
    if (n == 1) { ra[3 * 64] = acc[3]; asm volatile("" ::: "memory"); }
    if (n == 2) {
      asm volatile("s_waitcnt lgkmcnt(0)\n\ts_barrier" :: "v"(acc[3]) : "memory");
      ST(1, 8);
    }
    if (n == 3) { ub[0] = opaque(C.bp + (a & 1) * 768); ub[1] = opaque(C.bp - (a & 1) * 256); }
    if (n == 4) { tp = opaque((u32)((a >> 1) * 4 * CSTR + lane * 16)); bq = opaque((u32)(L_BD + lane * 16)); }
    if (n == 5) { C.ah = opaque(C.ah + (a & 1) * 256); C.ah2 = opaque(C.ah2 + (a & 1) * 256); }
    if (n >= 6 && n < 10) F_LU(F, 0, n - 6, (const LAS char*)(size_t)ub[(n - 6) >> 1] + 192);
    if (n >= 6 && n < 8) F.bd[n - 6] = *(const LAS f16x8*)(size_t)(bq + (n - 6) * 1024);
    if (n == PFS2 || n == PFS2 + PFD) prefill_o2(wrs, a, lane, n != PFS2);
    if (n >= 16 && n < 20) F.t[19 - n] = *(const LAS f16x8*)(size_t)(tp + (19 - n) * CSTR);
    if (n == 20) LDA(F.k4, 2048);
    if (n >= 21 && n < 25) { F_CV(F, 2 * (n - 21)); F_CV(F, 2 * (n - 21) + 1); }
  }
#define FM "v_mfma_f32_16x16x32_f16 "
  asm volatile("s_bitcmp1_b32 %13, 0\n\ts_cbranch_scc1 0f\n\t"
               FM "%3, %4, %9, 0\n\t" FM "%2, %4, %8, %2\n\t" FM "%1, %4, %7, %1\n\t" FM "%0, %4, %6, %0\n\t" FM "%3, %5, %6, %3\n\t" FM "%0, %10, %11, %0\n\t" FM "%3, %10, %12, %3\n\ts_branch 1f\n0:\n\t"
               FM "%3, %4, %8, 0\n\t" FM "%2, %4, %7, %2\n\t" FM "%1, %4, %6, %1\n\t" FM "%2, %5, %6, %2\n\t" FM "%3, %5, %7, %3\n\t" FM "%1, %10, %11, %1\n\t" FM "%2, %10, %12, %2\n1:"
               : "+v"(acc[4]), "+v"(acc[5]), "+v"(acc[6]), "=&v"(acc[7])
               : "v"(F.v[0]), "v"(F.v[1]), "v"(F.t[0]), "v"(F.t[1]), "v"(F.t[2]), "v"(F.t[3]), "v"(F.k4), "v"(F.u[0][1]), "v"(F.u[1][1]), "s"(a) : "scc");
  ST(1, 5);
  asm volatile("s_waitcnt vmcnt(0)\n\ts_barrier" ::: "memory");
  ST(1, 6);
  tri(4);
  asm volatile("s_nop 7" : "+v"(acc[7]));
  ra[1024 + 3 * 64] = acc[7];
  asm volatile("s_waitcnt lgkmcnt(0)\n\ts_barrier" :: "v"(acc[7]) : "memory");
  ST(1, 9);
}

struct C1Pre { u32 ah, ah2, bp, xp, xc, so, hv, cop; };
__device__ __forceinline__ void c1_pre(C1Pre& C, int kp, int a, int lane) {
  lane = (int)opaque((u32)lane);
  const int c = a >> 1, P = a & 1, j16 = lane & 15, kg = lane >> 4;
  const int y0 = 2031 - j16 + 8 * kg, sg = y0 & 3;
  C.ah = c * 4 * CSTR + sg * CSTR + 2 * (y0 - sg) - 4032; C.ah2 = C.ah + 8;
  C.bp = L_U + c * 4608 + (240 + 16 * j16 + 8 * kg) * 2 - 448;
  C.xp = L_X + j16 * 64 + kg * 16; C.xc = L_XC + c * 4096 + lane * 16;
  C.so = OFF_X1 + (16 * ((kg & 1) ? 3 - P : P) + (j16 & ~1) + c) * XBS + kp * 64 + ((j16 & 1) ^ c) * 32 + (kg >> 1) * 16;
  C.hv = L_KT + c * TBS + 2 * (0x4480 - 0x3F00 - 6 + 64 * P + 2 * j16 - 8 * kg);
  asm volatile("" : "+v"(C.ah), "+v"(C.ah2), "+v"(C.bp), "+v"(C.xp), "+v"(C.xc), "+v"(C.so), "+v"(C.hv));
}
template <int P>
__device__ __forceinline__ void a_conv1(char* smem, rsrc_t wrs, int kp, int a, int lane, const C1Pre& C, u32x4& fl, u32x4& wfl, u32& g2) {
  if (P) PAGEV(2); else PAGEW(1);
  const int c = a >> 1;
  const LAS char *ah = (const LAS char*)(size_t)C.ah, *ah2 = (const LAS char*)(size_t)C.ah2, *bp = (const LAS char*)(size_t)C.bp, *xp = (const LAS char*)(size_t)C.xp;
  LAS char* xc = (LAS char*)(size_t)C.xc;
  constexpr int CX = P, CY = 3 - P, NMU = 4 - P;
  int so = (int)C.so, fo0, fo1;
  const u32 mi = opaque(L_MI), mx = opaque(L_MI + 4 * c);
  u32 hw[2][5], hv = 0, h1 = 0;
#pragma unroll
  for (int e = 0; e < 10; ++e) hw[e / 5][e % 5] = *(const LAS u32*)(size_t)(C.hv + (e / 5) * 64 + (e % 5) * 4);
  if (P) { const LAS u16* q = (const LAS u16*)(size_t)(u32)(L_KT + c * TBS + 2 * (0x4480 - 0x3F00)); hv = q[0]; h1 = q[-1]; }
  const u32 sel = c ? 0x03020c0cu : 0x01000c0cu;
  const float isc = *(const float*)(smem + L_MI + 4 * c);
  f16x8 fa[8][5], fb[8][8];
  Far F;
  u32 bq;
  f32x4 X, Y, acc[4], pre[2], pq[2];
  u32x4 xr[2];
  u32 kh = 0;
#define LDA1(r_, mu_) do { const int ao_ = 4032 - 64 * (r_) - 512 * (mu_);                                             \
      f16x4 h0_ = *(const LAS f16x4*)(ah + ao_), h1_ = *(const LAS f16x4*)(ah2 + ao_);                                \
      fa[r_][mu_] = __builtin_shufflevector(h0_, h1_, 0, 1, 2, 3, 4, 5, 6, 7); __builtin_amdgcn_sched_barrier(0); } while (0)
#define LDB1(r_, t_) fb[r_][t_] = *(const LAS f16x8*)(bp + 512 * (t_) + 448 - 64 * (r_))
#define MF(z_, d_, r_, mu_, t_) do { if (z_) asm volatile("v_mfma_f32_16x16x32_f16 %0, %1, %2, 0" : "=&v"(d_) : "v"(fa[r_][mu_]), "v"(fb[r_][t_])); \
      else asm volatile("v_mfma_f32_16x16x32_f16 %0, %1, %2, %0" : "+v"(d_) : "v"(fa[r_][mu_]), "v"(fb[r_][t_])); } while (0)
#define SBR() __builtin_amdgcn_sched_barrier(0)
#define RO(i_) (((i_) + 4 * (1 - P)) & 7)
  auto tri = [&](auto H_) __attribute__((always_inline)) {
    constexpr int H = decltype(H_)::value, T = 4 * H;
    if (H == 0) {
#pragma unroll
      for (int i = 0; i < 2; ++i)
#pragma unroll
        for (int e = 0; e < NMU; ++e) { LDB1(RO(i), T + e); SBR(); }
    }
#pragma unroll
    for (int i = 0; i < 8; ++i) {
      const int r = RO(i), rn = RO((i + 2) & 7);
#pragma unroll
      for (int e = 0; e < 5; ++e) {
        const bool z = H == 0 && i == 0;
        if (e == 0 && !z) asm volatile("" : "+v"(fb[r][T + NMU - 1]));
        if (P == 0) { if (e == 0) MF(z, Y, r, 3, T); else if (e == 1) MF(z, X, r, 0, T); else MF(false, Y, r, 4 - e, T + e - 1); }
        else { if (e == 0) MF(z, Y, r, 2, T); else if (e == 1) MF(z, X, r, 1, T); else if (e == 2) MF(false, X, r, 0, T + 1); else if (e == 3) MF(false, Y, r, 1, T + 1); else MF(false, Y, r, 0, T + 2); }
        SBR();
        if (i < 6 && e < NMU) LDB1(rn, T + e);
        if (H == 0 && i >= 1 && i <= NMU && e == 4) LDA1(RO(7), i - 1);
#define CNT_LD(c_) kh = *(const volatile LAS u32*)(size_t)(mi + (c_))
#define CNT_CK(c_, f_) if (__builtin_expect(__builtin_amdgcn_readfirstlane(kh) < 4u, 0)) { FAILC(f_); cnt_spin(smem, c_, 4); }
        if (i == 2 * H && e == 4) { xr[0] = *(const LAS u32x4*)(xp + (T + CX) * 1024); xr[1] = *(const LAS u32x4*)(xp + (T + CY) * 1024); }
        if (H == 0) {
          if (i == 3 && e == 1) CNT_LD(C_KH);
          if (i == 5 && e == 4) CNT_CK(C_KH, 17);
          if (i == 7 && e == 0 && P) LDB1(RO(7), 3);
          if (i == 7 && e == 1 && P) LDA1(RO(7), 3);
        } else {
          if (i == 0 && e == 1) *(LAS f32x4*)(xc + P * 2048) = acc[1 - P];
          if (i == 0 && e == 4) { *(LAS f32x4*)(xc + P * 2048 + 1024) = acc[2 + P]; asm volatile("" :: "v"(acc[1 - P]) : "memory"); cnt_add(smem, C_XA + 4 * c, lane); }
          if (i == 1 && e == 1) asm volatile("" :: "v"(acc[2 + P]));
          if (i == 1 && e == 4) { fl = ldw(wrs, fo0); wfl = ldw(wrs, fo1); }
          if (i == 3 && e == 1) kh = *(const volatile LAS u32*)(size_t)(mx + C_XA);
          if (i == 4 && e == 4) {
            if (__builtin_expect(__builtin_amdgcn_readfirstlane(kh) < 2u, 0)) { FAILC(22); cnt_spin(smem, C_XA + 4 * c, 2); }
          }
          if (i == 4 && e == 4) { pq[0] = *(const LAS f32x4*)(xc + (1 - P) * 2048); pq[1] = *(const LAS f32x4*)(xc + (1 - P) * 2048 + 1024); }
          if (i == 6 && e == 2) CNT_LD(C_P3);
          if (i == 7 && e == 3) { CNT_CK(C_P3, 12); g2 = *(const volatile LAS u32*)(size_t)(mi + C_G2); }
        }
        if (i >= 6 && i < 7 + H && e < 4) {
#pragma unroll
          for (int t = 0; t < 2; ++t) {
            pre[t][e] = i == 7 ? fmaf(pq[t][e], isc, pre[t][e]) : __uint_as_float(__builtin_amdgcn_perm(xr[t][e], xr[t][e], sel));
            asm volatile("" : "+v"(pre[t][e]));
          }
        }
        SBR();
      }
    }
    u32 o[4];
    float t0, t1;
    asm volatile("v_fma_f32 %0, %6, %22, %14\n\tv_fma_f32 %4, %7, %22, %15\n\tv_fma_f32 %1, %8, %22, %16\n\tv_fma_f32 %5, %9, %22, %17\n\t"
                 "v_cvt_pk_bf16_f32 %0, %0, %4\n\tv_cvt_pk_bf16_f32 %1, %1, %5\n\ts_nop 1\n\t"
                 "v_fma_f32 %2, %10, %22, %18\n\tv_fma_f32 %4, %11, %22, %19\n\tv_fma_f32 %3, %12, %22, %20\n\tv_fma_f32 %5, %13, %22, %21\n\t"
                 "v_cvt_pk_bf16_f32 %2, %2, %4\n\tv_cvt_pk_bf16_f32 %3, %3, %5\n\ts_nop 0\n\t"
                 "v_permlane16_swap_b32 %0, %2\n\tv_permlane16_swap_b32 %1, %3"
                 : "=&v"(o[0]), "=&v"(o[1]), "=&v"(o[2]), "=&v"(o[3]), "=&v"(t0), "=&v"(t1)
                 : "v"(X[0]), "v"(X[1]), "v"(X[2]), "v"(X[3]), "v"(Y[0]), "v"(Y[1]), "v"(Y[2]), "v"(Y[3]),
                   "v"(pre[0][0]), "v"(pre[0][1]), "v"(pre[0][2]), "v"(pre[0][3]), "v"(pre[1][0]), "v"(pre[1][1]), "v"(pre[1][2]), "v"(pre[1][3]), "v"(isc));
    const u32x4 ov = {o[0], o[1], o[2], o[3]};
    stw(wrs, so, ov);
    if (H == 0) cnt_add(smem, C_S, lane);
    asm volatile("s_nop 1" :: "v"(ov) : "memory");
    SBR();
  };
#pragma unroll
  for (int i = 0; i < 7; ++i)
#pragma unroll
    for (int e = 0; e < NMU; ++e) { LDA1(RO(i), e); SBR(); }
  {
    const u64 tm[4] = {0xf000ff00fff0ffffull, 0xe000fe00ffe0fffeull, 0xc000fc00ffc0fffcull, 0x8000f800ff80fff8ull};
    u32x4 t[2];
#pragma unroll
    for (int e = 0; e < 8; ++e) {
      const u32 w_ = __builtin_amdgcn_perm(hw[e >> 2][3 - (e & 3)], hw[e >> 2][4 - (e & 3)], 0x07060100u);
      t[e >> 2][e & 3] = P == 0 && e < 4 && !__builtin_amdgcn_inverse_ballot_w64(tm[e & 3]) ? 0u : w_;
    }
    asm volatile("" : "+v"(t[0]), "+v"(t[1]));
    LAS u32x4* q = (LAS u32x4*)(size_t)(C.cop + P * (2 * CSTR - 1024));
    q[0] = t[0]; q[CSTR / 16] = t[1];
    asm volatile("s_nop 1" :: "v"(t[0]), "v"(t[1]) : "memory");
    if (P) {
      const bool z = __builtin_amdgcn_inverse_ballot_w64(0x8888888888888888ull);
      const u32 vv = z ? __builtin_amdgcn_perm(hv, hv, 0x01000100u) : 0u, vw = z ? __builtin_amdgcn_perm(h1, hv, 0x05040100u) : 0u, ut = vv & 0xFFFF0000u;
      put_copies((u32)(c * 4 * CSTR + (124 + (lane & 3)) * 16), (u32x4){0, ut, vv, vv}, (u32x4){0, vv, vv, vv}, (u32x4){ut, vv, vv, vv}, (u32x4){vv, vv, vv, vw});
    }
    cnt_add(smem, C_KH, lane);
  }
  ST(0, 6);
  cnt_spin(smem, C_U, 4);
  ST(0, 7);
  __builtin_amdgcn_s_setprio(1);
  tri(IC<0>{});
  ST(0, 8);
  auto ab = [&](auto N_) __attribute__((always_inline)) {
    constexpr int n = decltype(N_)::value, nd = n < 12 ? n : n >= 16 && n < 28 ? n - 4 : -1, rr = nd < 0 ? 0 : nd / 6, k = nd < 0 ? 0 : nd % 6;
    constexpr int mu = k < 1 ? 1 : k < 3 ? 2 : 3, cb = k - mu * (mu - 1) / 2, q = n < 25 ? 0 : n - 25;
    if (nd >= 0) MF(rr == 0 && cb + 4 - mu == 3, acc[cb], RO(7 - rr), mu, cb + 4 - mu);
    else if (n < 16) F_WS(F, n & 3);
    else F_MM(F, P, (n - 28) & 7, acc);
    SBR();
    if (n == 0) { bq = opaque((u32)(L_BD + lane * 16)); if (P) LDB1(RO(6), 3); }
    if (n == 1 && P) LDA1(RO(6), 3);
    if (n >= 2 && n < 4) { F_LU(F, P, n & 1, bp + 448); F.bd[n & 1] = *(const LAS f16x8*)(size_t)(bq + (n & 1) * 1024); }
    if (n >= 4 && n < 6) F_LU(F, P, (n - 2) & 3, bp + 448);
    if (n >= 7 && n < 10) LDB1(RO(5), (10 - n) & 3);
    if (n >= 13 && n < 16) LDB1(RO(4), (16 - n) & 3);
    if ((n == 10 || n == 16) && P) LDA1(RO(n == 10 ? 5 : 4), 3);
    if (n == 6) { so += 64 * XBS; asm volatile("" : "+v"(so)); }
    if (n == 11) fo0 = (int)opaque((u32)(OFF_ST0 + (a * 64 + lane) * 16));
    if (n == 12) fo1 = (int)opaque((u32)(OFF_ST0 + 8192 + ((kp & 7) * 4 + a) * 128 + (lane & 3) * 16));
    if (n == 17) CNT_LD(C_P2);
    if (n >= 18 && n < 22 - P) F.t[(21 - P - n) & 3] = *(const LAS f16x8*)(size_t)(C.cop + ((21 - P - n) & 3) * CSTR - P * 1024);
    if (n >= 17 && n < 25) F_CV(F, (n - 17) & 7);
    if (n == 22) { LDA1(0, 4); F.k4 = fa[0][4]; }
    if (n == 24) CNT_CK(C_P2, 23);
    if (n >= 25 && q < 2 * NMU) LDB1(RO(q / NMU), 4 + q % NMU);
    SBR();
  };
  static_for<0, 35>(ab);
  X = acc[CX]; Y = acc[CY];
  tri(IC<1>{});
  __builtin_amdgcn_s_setprio(0);
  ST(0, 9);
}

__device__ __forceinline__ float vsel_(float a, float b, u64 m) { float d; asm volatile("v_cndmask_b32_e64 %0, %1, %2, %3" : "=v"(d) : "v"(a), "v"(b), "s"(m)); return d; }
struct XPre { u32 p, q[2], spw, six; u32x4 one; u64 m[3]; };
__device__ __forceinline__ void x_pre(XPre& X, int H, int a, int lane, bool pb) {
  X.m[0] = 0xAAAAAAAAAAAAAAAAull; X.m[1] = 0xCCCCCCCCCCCCCCCCull; X.m[2] = 0xf0000f0000f0000full;
  X.p = (H ? L_TB : L_TA) + a * 4096 + lane * 8;
#pragma unroll
  for (int e = 0; e < 3; ++e) asm volatile("" : "+s"(X.m[e]));
  X.spw = L_SP + H * 1536 + (lane & 15) * 16 + a * 4 + (__builtin_amdgcn_inverse_ballot_w64(X.m[2]) ? 768 : 0);
  X.six = __builtin_amdgcn_inverse_ballot_w64(X.m[0]) ? 0xEEEEu : 0x4444u;
  X.one = (u32x4){0x3F803F80u, 0x3F803F80u, 0x3F803F80u, 0x3F803F80u};
  asm volatile("" : "+v"(X.p), "+v"(X.spw), "+v"(X.six), "+v"(X.one));
  if (pb) { X.q[0] = X.p + (lane & 4) * 4096; X.q[1] = X.q[0] ^ 32u; asm volatile("" : "+v"(X.q[0]), "+v"(X.q[1])); }
}
template <int HB, int NY, bool PB>
__device__ __forceinline__ void x_take(char* smem, rsrc_t wrs, int xin, int rb16, int a, int lane, const XPre& X, u32 sent) {
  constexpr int tile = HB ? L_TB : L_TA, H = HB && !(PB ? EX1 : EX2), NT = H ? 3 : 2;
  typedef __attribute__((ext_vector_type(16))) short s16;
#define XKO(t_) ((t_) == 2 ? L_TH - L_TB : (t_) * 16384)
#define XTL(o_) (H ? (PB ? ((o_) + 2) % 3 : (o_) == 0 ? 0 : 3 - (o_)) : 1 - (o_))
  float s = 0.f;
  f32x4 ss[3], sg[3];
  bf16x8 xf[4];
  auto rd = [&](int o) __attribute__((always_inline)) {
#pragma unroll
    for (int j = 0; j < 4; ++j)
      xf[j] = ld_tr8(PB && o >= H ? (const LAS char*)(size_t)X.q[XTL(H + (j >> 1))] + (2 * (o - H) + (j & 1)) * 1024 : (const LAS char*)(size_t)X.p + XKO(XTL(o)) + j * 1024);
    __builtin_amdgcn_sched_barrier(0);
    if (!PB || o <= H) { ss[o] = (f32x4){0.f, 0.f, 0.f, 0.f}; asm volatile("" : "+v"(ss[o])); }
    if (PB && o == H) { ss[o + 1] = (f32x4){0.f, 0.f, 0.f, 0.f}; asm volatile("" : "+v"(ss[o + 1])); }
  };
  auto ex = [&](int o) __attribute__((always_inline)) {
    float d0, d1;
    if (o == NT - 1 && !PB) asm volatile("s_nop 3");
    asm volatile("v_cndmask_b32_e64 %0, %3, %4, %9\n\tv_cndmask_b32_e64 %1, %5, %6, %9\n\tv_cndmask_b32_e64 %0, %0, %1, %10\n\tv_add_f32 %2, %2, %0\n\t"
                 "v_add_f32 %1, %7, %8\n\tv_cndmask_b32_e64 %1, %1, %0, %11"
                 : "=&v"(d0), "=&v"(d1), "+v"(s) : "v"(sg[o][0]), "v"(sg[o][1]), "v"(sg[o][2]), "v"(sg[o][3]), "v"(ss[o][0]), "v"(ss[o][1]), "s"(X.m[0]), "s"(X.m[1]), "s"(X.m[2]), "v"(ss[o]));
    *(LAS float*)(size_t)(X.spw + XTL(o) * 256) = d1;
    __builtin_amdgcn_sched_barrier(0);
  };
  auto mm = [&](int o) __attribute__((always_inline)) {
    const bool u = PB && o >= H;
#pragma unroll
    for (int j = 0; j < 4; ++j) {
      const int q = u ? H + (j >> 1) : o;
      if (u ? o > H || (j & 1) : j) asm volatile("v_mfma_f32_16x16x32_bf16 %0, %1, %1, %0" : "+v"(sg[q]) : "v"(xf[j]));
      else asm volatile("v_mfma_f32_16x16x32_bf16 %0, %1, %1, 0" : "=&v"(sg[q]) : "v"(xf[j]));
      if (j & 1) asm volatile("v_smfmac_f32_16x16x64_bf16 %0, %1, %2, %3" : "+v"(ss[q]) : "v"(X.one), "v"((s16)__builtin_shufflevector(xf[j - 1], xf[j], 0, 1, 2, 3, 4, 5, 6, 7, 8, 9, 10, 11, 12, 13, 14, 15)), "v"(X.six));
    }
    __builtin_amdgcn_sched_barrier(0);
  };
  unsigned n = 0;
  u64 chk = ~0ull, bad;
  asm volatile("" : "+s"(n));
#pragma unroll 1
  do {
    if (n++) {
      FAILC((xin == OFF_X1 ? 1 : 9) + HB);
      if (xin == OFF_X1) __builtin_amdgcn_s_sleep(HB ? SLP_XR2 : SLP_XR1); else __builtin_amdgcn_s_sleep(HB ? SLP_XR4 : SLP_XR3);
      x_dma(wrs, xin, rb16, tile, a, lane);
      if (NY) asm volatile("s_waitcnt vmcnt(0)" ::: "memory");
      if (n > 4) {
        asm volatile("s_waitcnt vmcnt(0)" ::: "memory");
        bool st = false;
#pragma unroll 1
        for (int e = 0; e < 4 * NT; ++e) { const u32x4 v = *(const u32x4*)(smem + tile + a * 4096 + lane * 16 + XKO(e >> 2) + (e & 3) * 1024); st |= v[1] == sent || v[3] == sent; }
        if (!__any(st) || n > SPIN_CAP) chk = 0;
      }
    }
    asm volatile("s_waitcnt vmcnt(%0)" :: "n"(4 * (NT - 1) + NY) : "memory");
    s = 0.f;
    rd(0); mm(0);
#define XBAD() (__builtin_amdgcn_ballot_w64(s != s) & chk)
    bad = 0;
    constexpr bool EG = NT == 3 || (HB && !PB && XEG);
    if (EG) { asm volatile("s_nop 3"); ex(0); bad = XBAD(); }
    if (!bad) {
      asm volatile("s_waitcnt vmcnt(%0)" :: "n"(4 * (NT - 2) + NY) : "memory");
      rd(1);
      if (!EG && !PB) ex(0);
      mm(1);
      if (NT == 3) { asm volatile("s_waitcnt vmcnt(%0)" :: "n"(NY) : "memory"); rd(2); if (!PB) ex(1); mm(2); }
      if (PB) ex(NT - 2);
      ex(NT - 1);
      bad = XBAD();
    }
  } while (__builtin_expect(bad != 0, 0));
}
#define PIN(x_) asm volatile("" : "+v"(x_))
struct HaloMsg { u32x4 k, v; };
__device__ __forceinline__ void halo_ld(HaloMsg& h, rsrc_t wrs, int ho) { h.k = ldw(wrs, ho); h.v = ldw(wrs, ho + 1024); }
__device__ __forceinline__ void attention(u32x4 (&r)[3], HaloMsg& hm, int ho, u32 tag, rsrc_t wrs, int oo, bool first, int lane, u32x4 on, int fid) {
  const u64 VM0 = 0x0fff00ff000f0000ull, VM1 = 0x3fff03ff003f0003ull, VM2 = 0xf000ff00fff0ffffull, VM3 = 0xc000fc00ffc0fffcull;
  u64 PAR = 0xAAAAAAAAAAAAAAAAull, VF0 = first ? 0ull : VM0, VF1 = first ? 0ull : VM1, chk = ~0ull, bad;
  const u32 sl = (lane & 1) ? 0x01000c0cu : 0x0c0c0100u, su = (lane & 1) ? 0x03020c0cu : 0x0c0c0302u;
  u32 ni = 0xff800000u, sc = 0x3eb8aa3bu;
  asm volatile("" : "+s"(PAR));
  asm volatile("" : "+s"(sc));
  asm volatile("" : "+v"(ni));
  f32x4 l0, sh;
#define ATMP "v224", "v225", "v226", "v227", "v232", "v233", "v234", "v235", "v236", "v237", "v238", "v239"
  asm volatile(
      "v_mfma_f32_16x16x16_bf16 v[224:227], v[208:209], v[216:217], 0\n\t"
      "v_mfma_f32_16x16x16_bf16 v[220:223], v[218:219], v[216:217], 0\n\t"
      "v_mfma_f32_16x16x16_bf16 v[228:231], v[218:219], v[214:215], 0\n\t"
      "v_cndmask_b32_e64 v224, v224, v225, %[par]\n\t"
      "v_cndmask_b32_e64 v225, v226, v227, %[par]\n\t"
      "v_cndmask_b32_e64 v224, %[ni], v224, %[m2]\n\t"
      "v_cndmask_b32_e64 v225, %[ni], v225, %[m3]\n\t"
      "v_cndmask_b32_e64 v220, v220, v221, %[par]\n\t"
      "v_cndmask_b32_e64 v221, v222, v223, %[par]\n\t"
      "v_cndmask_b32_e64 v220, %[ni], v220, %[m0]\n\t"
      "v_cndmask_b32_e64 v221, %[ni], v221, %[m1]\n\t"
      "v_max_f32 v222, v224, v225\n\t"
      "v_max3_f32 v223, v222, v220, v221\n\t"
      "v_max3_f32 v222, v222, v220, v221\n\t"
      "v_cndmask_b32_e64 v228, v228, v229, %[par]\n\t"
      "v_cndmask_b32_e64 v229, v230, v231, %[par]\n\t"
      "v_permlane16_swap_b32 v222, v223\n\t"
      "v_max_f32 v226, v222, v223\n\t"
      "v_max_f32 v227, v222, v223\n\t"
      "v_cndmask_b32_e64 v228, %[ni], v228, %[m2]\n\t"
      "v_cndmask_b32_e64 v229, %[ni], v229, %[m3]\n\t"
      "v_permlane32_swap_b32 v226, v227\n\t"
      "v_max_f32 v222, v226, v227\n\t"
      "v_mul_f32_e64 v222, -v222, %[sc]\n\t"
      "v_fma_f32 v224, v224, %[sc], v222\n\t"
      "v_fma_f32 v225, v225, %[sc], v222\n\t"
      "v_exp_f32 v224, v224\n\t"
      "v_exp_f32 v225, v225\n\t"
      "v_fma_f32 v220, v220, %[sc], v222\n\t"
      "v_fma_f32 v221, v221, %[sc], v222\n\t"
      "v_exp_f32 v220, v220\n\t"
      "v_exp_f32 v221, v221\n\t"
      "v_cvt_pk_bf16_f32 v226, v224, v225\n\t"
      "v_cvt_pk_bf16_f32 v227, v220, v221\n\t"
      "v_perm_b32 v232, v226, v226, %[sl]\n\t"
      "v_perm_b32 v233, v226, v226, %[su]\n\t"
      "v_perm_b32 v234, v227, v227, %[sl]\n\t"
      "v_perm_b32 v235, v227, v227, %[su]\n\t"
      "s_nop 1\n\t"
      "v_mfma_f32_16x16x32_bf16 v[224:227], %[on], v[232:235], 0\n\t"
      "v_mfma_f32_16x16x32_bf16 v[236:239], v[210:213], v[232:235], 0\n\t"
      "s_nop 3\n\t"
      "v_rcp_f32 v222, v224\n\t"
      "s_nop 2\n\t"
      "v_mul_f32 v236, v236, v222\n\t"
      "v_mul_f32 v237, v237, v222\n\t"
      "v_mul_f32 v238, v238, v222\n\t"
      "v_mul_f32 v239, v239, v222\n\t"
      "v_cvt_pk_bf16_f32 v218, v236, v237\n\t"
      "v_cvt_pk_bf16_f32 v219, v238, v239\n\t"
      "v_max_f32 v230, v228, v229\n\t"
      : "+{v[216:219]}"(r[1]), "=&{v[228:231]}"(l0)
      : "{v[208:211]}"(r[0]), "{v[212:215]}"(r[2]), [par] "s"(PAR), [m0] "s"(VM0), [m1] "s"(VM1), [m2] "s"(VM2), [m3] "s"(VM3), [ni] "v"(ni), [sc] "s"(sc), [sl] "v"(sl), [su] "v"(su), [on] "v"(on)
      : "v220", "v221", "v222", "v223", ATMP, "memory");
  unsigned n = 0;
  asm volatile("" : "+s"(n), "+s"(VF0));
  asm volatile("" : "+s"(VF1));
#pragma unroll 1
  do {
    if (n++) {
      FAILC(fid);
      __builtin_amdgcn_s_sleep(SLP_HR);
      asm volatile("" ::: "memory");
      halo_ld(hm, wrs, ho);
      if (n > SPIN_CAP) chk = 0;
    }
    asm volatile(
      "v_bitop3_b32 v232, v241, v243, %[tag] bitop3:0x7e\n\t"
      "v_mov_b32 v241, v242\n\t"
      "v_bitop3_b32 v233, v245, v247, %[tag] bitop3:0x7e\n\t"
      "v_mov_b32 v210, v244\n\t"
      "v_mfma_f32_16x16x16_bf16 v[220:223], v[240:241], v[214:215], 0\n\t"
      "v_mov_b32 v211, v246\n\t"
      "v_or_b32 v232, v232, v233\n\t"
      "v_cmp_ne_u32_e64 %[bad], 0, v232\n\t"
      : "+{v[240:243]}"(hm.k), "+{v[208:211]}"(r[0]), "=&{v[220:223]}"(sh), [bad] "=&s"(bad) : "{v[244:247]}"(hm.v), "{v[212:215]}"(r[2]), [tag] "s"(tag) : "v232", "v233");
    bad &= chk;
  } while (__builtin_expect(bad != 0, 0));
  asm volatile(
      "s_nop 2\n\t"
      "v_cndmask_b32_e64 v220, v220, v221, %[par]\n\t"
      "v_cndmask_b32_e64 v221, v222, v223, %[par]\n\t"
      "v_cndmask_b32_e64 v220, %[ni], v220, %[m0]\n\t"
      "v_cndmask_b32_e64 v221, %[ni], v221, %[m1]\n\t"
      "v_max3_f32 v223, v230, v220, v221\n\t"
      "v_max3_f32 v222, v230, v220, v221\n\t"
      "s_nop 1\n\t"
      "v_permlane16_swap_b32 v222, v223\n\t"
      "v_max_f32 v226, v222, v223\n\t"
      "v_max_f32 v227, v222, v223\n\t"
      "s_nop 1\n\t"
      "v_permlane32_swap_b32 v226, v227\n\t"
      "v_max_f32 v222, v226, v227\n\t"
      "v_mul_f32_e64 v222, -v222, %[sc]\n\t"
      "v_fma_f32 v220, v220, %[sc], v222\n\t"
      "v_fma_f32 v221, v221, %[sc], v222\n\t"
      "v_exp_f32 v220, v220\n\t"
      "v_exp_f32 v221, v221\n\t"
      "v_fma_f32 v228, v228, %[sc], v222\n\t"
      "v_fma_f32 v229, v229, %[sc], v222\n\t"
      "v_exp_f32 v228, v228\n\t"
      "v_exp_f32 v229, v229\n\t"
      "v_cvt_pk_bf16_f32 v226, v220, v221\n\t"
      "v_cvt_pk_bf16_f32 v227, v228, v229\n\t"
      "v_perm_b32 v232, v226, v226, %[sl]\n\t"
      "v_perm_b32 v233, v226, v226, %[su]\n\t"
      "v_perm_b32 v234, v227, v227, %[sl]\n\t"
      "v_perm_b32 v235, v227, v227, %[su]\n\t"
      "s_nop 1\n\t"
      "v_mfma_f32_16x16x32_bf16 v[224:227], %[on], v[232:235], 0\n\t"
      "v_mfma_f32_16x16x32_bf16 v[236:239], v[210:213], v[232:235], 0\n\t"
      "s_nop 3\n\t"
      "v_rcp_f32 v222, v224\n\t"
      "s_nop 2\n\t"
      "v_mul_f32 v236, v236, v222\n\t"
      "v_mul_f32 v237, v237, v222\n\t"
      "v_mul_f32 v238, v238, v222\n\t"
      "v_mul_f32 v239, v239, v222\n\t"
      "v_cvt_pk_bf16_f32 v216, v236, v237\n\t"
      "v_cvt_pk_bf16_f32 v217, v238, v239\n\t"
      "s_nop 0\n\t"
      "v_permlane16_swap_b32 v216, v218\n\t"
      "v_permlane16_swap_b32 v217, v219\n\t"
      : "+{v[216:219]}"(r[1]), "+{v[228:231]}"(l0), "+{v[220:223]}"(sh)
      : "{v[208:211]}"(r[0]), "{v[212:215]}"(r[2]), [par] "s"(PAR), [m0] "s"(VF0), [m1] "s"(VF1), [ni] "v"(ni), [sc] "s"(sc), [sl] "v"(sl), [su] "v"(su), [on] "v"(on)
      : ATMP);
  stw(wrs, oo, r[1]);
  asm volatile("s_nop 1" :: "v"(r[1]) : "memory");
}
struct GCst { u64 PAR, VM[4]; u32 sc; };
__device__ __forceinline__ void attention_g(u32x4 (&r)[3], HaloMsg& hm, int ho, int hso, u32 tag, rsrc_t wrs, int oo, u32 sl, u32 su, u32 ni, u32x4 on, const GCst& K, int fid, bool late, f32x4 aq, f32x4 av, f32x4 tq, f32x4 tv, f32x4 rv, float rn) {
  const u64 PAR = K.PAR, VM0 = K.VM[0], VM1 = K.VM[1], VM2 = K.VM[2], VM3 = K.VM[3], VF0 = VM0, VF1 = VM1;
  const u32 sc = K.sc;
  u64 chk = FK ? 0ull : ~0ull, bad;
  asm volatile("" : "+s"(chk));
  f32x4 l0, sh;
#define ATMG "v24", "v25", "v26", "v27", "v32", "v33", "v34", "v35", "v36", "v37", "v38", "v39"
  asm volatile(
      "v_mfma_f32_16x16x16_bf16 v[24:27], v[8:9], v[16:17], 0\n\t"
      "v_mfma_f32_16x16x16_bf16 v[20:23], v[18:19], v[16:17], 0\n\t"
      "v_fma_f32 v14, %[aq0], %[rn], %[tq0]\n\t"
      "v_fma_f32 v28, %[aq1], %[rn], %[tq1]\n\t"
      "v_fma_f32 v15, %[aq2], %[rn], %[tq2]\n\t"
      "v_fma_f32 v29, %[aq3], %[rn], %[tq3]\n\t"
      "v_cvt_pk_bf16_f32 v14, v14, v28\n\t"
      "v_cvt_pk_bf16_f32 v15, v15, v29\n\t"
      "v_cndmask_b32_e64 v24, v24, v25, %[par]\n\t"
      "v_cndmask_b32_e64 v25, v26, v27, %[par]\n\t"
      "v_mfma_f32_16x16x16_bf16 v[28:31], v[18:19], v[14:15], 0\n\t"
      "v_cndmask_b32_e64 v24, %[ni], v24, %[m2]\n\t"
      "v_cndmask_b32_e64 v25, %[ni], v25, %[m3]\n\t"
      "v_cndmask_b32_e64 v20, v20, v21, %[par]\n\t"
      "v_cndmask_b32_e64 v21, v22, v23, %[par]\n\t"
      "v_cndmask_b32_e64 v20, %[ni], v20, %[m0]\n\t"
      "v_cndmask_b32_e64 v21, %[ni], v21, %[m1]\n\t"
      "v_max_f32 v22, v24, v25\n\t"
      "v_max3_f32 v23, v22, v20, v21\n\t"
      "v_max3_f32 v22, v22, v20, v21\n\t"
      "v_cndmask_b32_e64 v28, v28, v29, %[par]\n\t"
      "v_cndmask_b32_e64 v29, v30, v31, %[par]\n\t"
      "v_permlane16_swap_b32 v22, v23\n\t"
      "v_max_f32 v26, v22, v23\n\t"
      "v_max_f32 v27, v22, v23\n\t"
      "v_cndmask_b32_e64 v28, %[ni], v28, %[m2]\n\t"
      "v_cndmask_b32_e64 v29, %[ni], v29, %[m3]\n\t"
      "v_permlane32_swap_b32 v26, v27\n\t"
      "v_max_f32 v22, v26, v27\n\t"
      "v_mul_f32_e64 v22, -v22, %[sc]\n\t"
      "v_fma_f32 v24, v24, %[sc], v22\n\t"
      "v_fma_f32 v25, v25, %[sc], v22\n\t"
      "v_exp_f32 v24, v24\n\t"
      "v_exp_f32 v25, v25\n\t"
      "v_fma_f32 v20, v20, %[sc], v22\n\t"
      "v_fma_f32 v21, v21, %[sc], v22\n\t"
      "v_exp_f32 v20, v20\n\t"
      "v_exp_f32 v21, v21\n\t"
      "v_fma_f32 v12, %[av0], %[rv0], %[tv0]\n\t"
      "v_fma_f32 v36, %[av1], %[rv1], %[tv1]\n\t"
      "v_fma_f32 v13, %[av2], %[rv2], %[tv2]\n\t"
      "v_fma_f32 v37, %[av3], %[rv3], %[tv3]\n\t"
      "v_cvt_pk_bf16_f32 v26, v24, v25\n\t"
      "v_cvt_pk_bf16_f32 v27, v20, v21\n\t"
      "v_perm_b32 v32, v26, v26, %[sl]\n\t"
      "v_perm_b32 v33, v26, v26, %[su]\n\t"
      "v_perm_b32 v34, v27, v27, %[sl]\n\t"
      "v_perm_b32 v35, v27, v27, %[su]\n\t"
      "v_cvt_pk_bf16_f32 v12, v12, v36\n\t"
      "v_cvt_pk_bf16_f32 v13, v13, v37\n\t"
      "v_mfma_f32_16x16x32_bf16 v[24:27], %[on], v[32:35], 0\n\t"
      "s_nop 0\n\t"
      "v_mfma_f32_16x16x32_bf16 v[36:39], v[10:13], v[32:35], 0\n\t"
      "s_nop 3\n\t"
      "v_rcp_f32 v22, v24\n\t"
      "s_nop 2\n\t"
      "v_mul_f32 v36, v36, v22\n\t"
      "v_mul_f32 v37, v37, v22\n\t"
      "v_mul_f32 v38, v38, v22\n\t"
      "v_mul_f32 v39, v39, v22\n\t"
      "v_cvt_pk_bf16_f32 v18, v36, v37\n\t"
      "v_cvt_pk_bf16_f32 v19, v38, v39\n\t"
      "v_max_f32 v30, v28, v29\n\t"
      : "+{v[16:19]}"(r[1]), "=&{v[28:31]}"(l0), "=&{v[12:15]}"(r[2])
      : "{v[8:11]}"(r[0]), [aq0] "v"(aq[0]), [aq1] "v"(aq[1]), [aq2] "v"(aq[2]), [aq3] "v"(aq[3]), [tq0] "v"(tq[0]), [tq1] "v"(tq[1]), [tq2] "v"(tq[2]), [tq3] "v"(tq[3]), [rn] "v"(rn),
        [av0] "v"(av[0]), [av1] "v"(av[1]), [av2] "v"(av[2]), [av3] "v"(av[3]), [tv0] "v"(tv[0]), [tv1] "v"(tv[1]), [tv2] "v"(tv[2]), [tv3] "v"(tv[3]),
        [rv0] "v"(rv[0]), [rv1] "v"(rv[1]), [rv2] "v"(rv[2]), [rv3] "v"(rv[3]), [par] "s"(PAR), [m0] "s"(VM0), [m1] "s"(VM1), [m2] "s"(VM2), [m3] "s"(VM3), [ni] "v"(ni), [sc] "s"(sc), [sl] "v"(sl), [su] "v"(su), [on] "v"(on)
      : "v20", "v21", "v22", "v23", ATMG, "memory");
  HaloMsg h2;
  if (late || DBL) {
    if (fid & 1) __builtin_amdgcn_s_sleep(SLP_G2); else __builtin_amdgcn_s_sleep(SLP_G1);
    h2.k = ldws(wrs, ho, hso); h2.v = ldws(wrs, ho + 1024, hso);
    if (late) hm = h2;
  }
  unsigned n = 0;
  asm volatile("" : "+s"(n));
#pragma unroll 1
  do {
    if (n++) {
      FAILC(fid);
      if (DBL && !late && n == 2) hm = h2;
      else {
        FAILC(fid - 6);
        __builtin_amdgcn_s_sleep(SLP_HG);
        asm volatile("" ::: "memory");
        hm.k = ldws(wrs, ho, hso); hm.v = ldws(wrs, ho + 1024, hso);
      }
      asm volatile("" : "+v"(hm.k), "+v"(hm.v));
      if (n > SPIN_CAP) chk = 0;
    }
    asm volatile(
      "v_bitop3_b32 v32, v41, v43, %[tag] bitop3:0x7e\n\t"
      "v_mov_b32 v41, v42\n\t"
      "v_bitop3_b32 v33, v45, v47, %[tag] bitop3:0x7e\n\t"
      "v_mov_b32 v10, v44\n\t"
      "v_mfma_f32_16x16x16_bf16 v[20:23], v[40:41], v[14:15], 0\n\t"
      "v_mov_b32 v11, v46\n\t"
      "v_or_b32 v32, v32, v33\n\t"
      "v_cmp_ne_u32_e64 %[bad], 0, v32\n\t"
      : "+{v[40:43]}"(hm.k), "+{v[8:11]}"(r[0]), "=&{v[20:23]}"(sh), [bad] "=&s"(bad) : "{v[44:47]}"(hm.v), "{v[12:15]}"(r[2]), [tag] "s"(tag) : "v32", "v33");
    bad &= chk;
  } while (__builtin_expect(bad != 0, 0));
  ST(1, 15);
  if (SIG == 2) cnt_add(nullptr, C_HR, 0); else asm volatile("s_nop 2");
  asm volatile(
      "v_cndmask_b32_e64 v20, v20, v21, %[par]\n\t"
      "v_cndmask_b32_e64 v21, v22, v23, %[par]\n\t"
      "v_cndmask_b32_e64 v20, %[ni], v20, %[m0]\n\t"
      "v_cndmask_b32_e64 v21, %[ni], v21, %[m1]\n\t"
      "v_max3_f32 v23, v30, v20, v21\n\t"
      "v_max3_f32 v22, v30, v20, v21\n\t"
      "s_nop 1\n\t"
      "v_permlane16_swap_b32 v22, v23\n\t"
      "v_max_f32 v26, v22, v23\n\t"
      "v_max_f32 v27, v22, v23\n\t"
      "s_nop 1\n\t"
      "v_permlane32_swap_b32 v26, v27\n\t"
      "v_max_f32 v22, v26, v27\n\t"
      "v_mul_f32_e64 v22, -v22, %[sc]\n\t"
      "v_fma_f32 v20, v20, %[sc], v22\n\t"
      "v_fma_f32 v21, v21, %[sc], v22\n\t"
      "v_exp_f32 v20, v20\n\t"
      "v_exp_f32 v21, v21\n\t"
      "v_fma_f32 v28, v28, %[sc], v22\n\t"
      "v_fma_f32 v29, v29, %[sc], v22\n\t"
      "v_exp_f32 v28, v28\n\t"
      "v_exp_f32 v29, v29\n\t"
      "v_cvt_pk_bf16_f32 v26, v20, v21\n\t"
      "v_cvt_pk_bf16_f32 v27, v28, v29\n\t"
      "v_perm_b32 v32, v26, v26, %[sl]\n\t"
      "v_perm_b32 v33, v26, v26, %[su]\n\t"
      "v_perm_b32 v34, v27, v27, %[sl]\n\t"
      "v_perm_b32 v35, v27, v27, %[su]\n\t"
      "s_nop 1\n\t"
      "v_mfma_f32_16x16x32_bf16 v[24:27], %[on], v[32:35], 0\n\t"
      "v_mfma_f32_16x16x32_bf16 v[36:39], v[10:13], v[32:35], 0\n\t"
      "s_nop 3\n\t"
      "v_rcp_f32 v22, v24\n\t"
      "s_nop 2\n\t"
      "v_mul_f32 v36, v36, v22\n\t"
      "v_mul_f32 v37, v37, v22\n\t"
      "v_mul_f32 v38, v38, v22\n\t"
      "v_mul_f32 v39, v39, v22\n\t"
      "v_cvt_pk_bf16_f32 v16, v36, v37\n\t"
      "v_cvt_pk_bf16_f32 v17, v38, v39\n\t"
      "s_nop 0\n\t"
      "v_permlane16_swap_b32 v16, v18\n\t"
      "v_permlane16_swap_b32 v17, v19\n\t"
      : "+{v[16:19]}"(r[1]), "+{v[28:31]}"(l0), "+{v[20:23]}"(sh)
      : "{v[8:11]}"(r[0]), "{v[12:15]}"(r[2]), [par] "s"(PAR), [m0] "s"(VF0), [m1] "s"(VF1), [ni] "v"(ni), [sc] "s"(sc), [sl] "v"(sl), [su] "v"(su), [on] "v"(on)
      : ATMG);
  stw(wrs, oo, r[1]);
  asm volatile("s_nop 1" :: "v"(r[1]) : "memory");
  if (DBL && !late) asm volatile("" :: "v"(h2.k), "v"(h2.v));
}
__device__ __forceinline__ void o_dma(rsrc_t wrs, int og, int ot, int a, int lane) {
  dma4k(wrs, lane * 16, og + a * 8192, ot + a * 8192);
  dma4k(wrs, lane * 16, og + a * 8192 + 4096, ot + a * 8192 + 4096);
}
template <bool L1, bool LATE, int OT, class F>
__device__ __forceinline__ void wo_mma2(f32x4 (&acc)[2], char* smem, rsrc_t wrs, int og, int a, int lane, const bf16x8 (&wof)[16], u32 cnt, const f32x4 (&r0)[2], const u32 (&qa)[4], u32 sent, F side) {
  constexpr int ot = OT;
  u32x4 of[32];
#define LDO2(s_) of[s_] = *(const LAS u32x4*)((const LAS char*)(size_t)qa[(s_) >> 3] + (ot - (L1 ? L_TA : L_O2)) + ((s_) & 7) * 1024)
#define WMM(s_) do { SB(); if ((s_) < 2) asm volatile("v_mfma_f32_16x16x32_bf16 %0, %1, %2, %3" : "=&v"(acc[(s_) & 1]) \
        : "v"(L1 ? __builtin_bit_cast(bf16x8, of[s_]) : wof[0]), "v"(L1 ? wof[0] : __builtin_bit_cast(bf16x8, of[s_])), "v"(r0[(s_) & 1])); \
      else if (L1) asm volatile("v_mfma_f32_16x16x32_bf16 %0, %1, %2, %0" : "+v"(acc[(s_) & 1]) : "v"(of[s_]), "v"(wof[(s_) >> 1])); \
      else asm volatile("v_mfma_f32_16x16x32_bf16 %0, %1, %2, %0" : "+v"(acc[(s_) & 1]) : "v"(wof[(s_) >> 1]), "v"(of[s_])); SB(); } while (0)
  u32 cv;
  unsigned n = 0;
  u64 chk = ~0ull, bad;
  asm volatile("" : "+s"(n));
#pragma unroll 1
  do {
    if (n++) {
      FAILC((L1 ? 5 : 13) + (ot != L_TA));
      __builtin_amdgcn_s_sleep(SLP_OR);
      o_dma(wrs, og, ot, a, lane);
      if (n > 4) {
        asm volatile("s_waitcnt vmcnt(0)" ::: "memory");
        bool st = false;
#pragma unroll 1
        for (int s = 0; s < 8; ++s) { const u32x4 v = *(const u32x4*)(smem + ot + a * 8192 + lane * 16 + s * 1024); st |= v[1] == sent || v[3] == sent; }
        if (!__any(st) || n > SPIN_CAP) chk = 0;
      }
    }
    asm volatile("s_waitcnt vmcnt(0)" ::: "memory");
    if (!L1 && !LATE) ST(1, 4);
    u32 mx = 0;
#pragma unroll
    for (int s = 0; s < 8; ++s) { LDO2(s); SB(); }
    side(-1);
#pragma unroll
    for (int s = 0; s < 3; ++s) {
      WMM(s);
#pragma unroll
      for (int e = 3 * s; e < 3 * s + 3 && e < 8; ++e) mx = max(max(mx, of[e][1]), of[e][3]);
      asm volatile("" : "+v"(mx));
    }
    bad = __builtin_amdgcn_ballot_w64(mx >= 0xFF810000u) & chk;
  } while (__builtin_expect(bad != 0, 0));
  if (LATE) {
    cnt_add(smem, C_OT, lane);
#pragma unroll
    for (int s = 3; s < WSP; ++s) WMM(s);
    side(7);
    cnt_spin(smem, C_OT, cnt);
#pragma unroll
    for (int s = 8; s < WSP + 8; ++s) { LDO2(s); SB(); }
#pragma unroll
    for (int s = WSP; s < 32; ++s) { WMM(s); if (s + 8 < 32) LDO2(s + 8); }
    return;
  }
  if constexpr (!L1) {
    lds_barrier();
#pragma unroll
    for (int s = 3; s < 8; ++s) {
      if (s < 7) { LDO2(2 * s + 2); LDO2(2 * s + 3); }
      WMM(s);
    }
  } else {
  cnt_add(smem, C_OT, lane);
  WMM(3);
  WMM(4);
  u32 c_, k_;
#define WR2(a_, b_) "ds_read_b128 %" #a_ ", %[q] offset:%[o]+" #a_ "*1024\n\tds_read_b128 %" #b_ ", %[q] offset:%[o]+" #b_ "*1024\n\t"
  asm volatile("ds_read_b32 %[cv], %[ca]\n\t" WR2(0, 1)
               "v_mfma_f32_16x16x32_bf16 %[a1], %[f5], %[w2], %[a1]\n\t" WR2(2, 3)
               "v_mfma_f32_16x16x32_bf16 %[a0], %[f6], %[w3], %[a0]\n\t" WR2(4, 5)
               "v_mfma_f32_16x16x32_bf16 %[a1], %[f7], %[w3], %[a1]\n\t"
               "s_brev_b32 %[k], 64\n\ts_branch 1f\n"
               "0:\n\tds_read_b32 %[cv], %[ca]\n\t" WR2(0, 1) WR2(2, 3) WR2(4, 5)
               "s_sub_u32 %[k], %[k], 1\n\ts_cbranch_scc1 2f\n"
               "1:\n\ts_waitcnt lgkmcnt(6)\n\tv_readfirstlane_b32 %[c], %[cv]\n\ts_cmp_lt_u32 %[c], %[n]\n\ts_cbranch_scc1 0b\n2:"
               : "=&v"(of[8]), "=&v"(of[9]), "=&v"(of[10]), "=&v"(of[11]), "=&v"(of[12]), "=&v"(of[13]), [cv] "=&v"(cv), [c] "=&s"(c_), [k] "=&s"(k_), [a0] "+v"(acc[0]), [a1] "+v"(acc[1])
               : [q] "v"(qa[1]), [o] "n"(OT - L_TA), [ca] "v"(L_MI + C_OT), [n] "s"(cnt), [f5] "v"(of[5]), [f6] "v"(of[6]), [f7] "v"(of[7]), [w2] "v"(wof[2]), [w3] "v"(wof[3]) : "scc", "memory");
  SB();
  LDO2(14); LDO2(15);
  }
#pragma unroll
  for (int s = 8; s < 32; ++s) {
    if (L1 && s < 14) asm volatile("s_waitcnt lgkmcnt(7)");
    if (!L1 && s >= 28) WMM(s == 29 ? 30 : s == 30 ? 29 : s); else WMM(s);
    if (s + 8 < 32) LDO2(s + 8);
    side(s);
  }
  if (L1) asm volatile("s_nop %2" : "+v"(acc[0]), "+v"(acc[1]) : "n"(5));
}
struct WoPre { f32x4 r[2]; int so; u32 ao, xs, c[3]; u64 m[2]; };
template <bool L1>
__device__ __forceinline__ void wo_pre(WoPre& P, const u32x2 (&res)[2], char* ws, int H, int rb, int hb, int a, int lane) {
  const int i16 = lane & 15, kg = lane >> 4, R0 = 1024 * H + 32 * rb;
#pragma unroll
  for (int t = 0; t < 2; ++t) P.r[t] = (f32x4){lo2f(res[t][0]), hi2f(res[t][0]), lo2f(res[t][1]), hi2f(res[t][1])};
  if (L1) {
    P.so = OFF_X2 + ((32 * hb + 8 * a + (i16 >> 1)) * XS_ + R0 + 16 * (lane & 1) + 4 * kg) * 4;
    P.ao = (u32)(OFF_ST2 + ((R0 & ~255) + ((R0 >> 2) & 63) + 4 * (lane & 1) + kg + ((lane >> 2) & 3) * 64) * 16 + ((lane >> 1) & 1) * 8);
    P.xs = L_XS + H * 512 + lane * 8;
    asm volatile("" : "+v"(P.r[0]), "+v"(P.r[1]), "+v"(P.so), "+v"(P.ao), "+v"(P.xs));
    P.c[0] = 0x05040100u; P.c[1] = 0x07060302u; P.c[2] = ~63u; P.m[0] = 0xAAAAAAAAAAAAAAAAull; P.m[1] = 0x3333333333333333ull;
#pragma unroll
    for (int e = 0; e < 3; ++e) asm volatile("" : "+s"(P.c[e]));
#pragma unroll
    for (int e = 0; e < 2; ++e) asm volatile("" : "+s"(P.m[e]));
  } else {
    P.so = ((R0 + 8 * a + (lane >> 3)) * 256 + 32 * hb + 4 * (lane & 7)) * 4;
    P.ao = H * 4096 + i16 * 128 + (((2 * a + (kg >> 1)) ^ i16) & 7) * 16 + (kg & 1) * 8;
    P.xs = H * 4096 + a * 1024 + (lane >> 3) * 128 + ((lane ^ (lane >> 3)) & 7) * 16;
    asm volatile("" : "+v"(P.r[0]), "+v"(P.r[1]), "+v"(P.so), "+v"(P.ao), "+v"(P.xs));
  }
}
__device__ __forceinline__ void out_put(const f32x4 (&acc)[2], u32 ad) {
  u32x2 p0, p1;
  asm volatile("v_cvt_pk_bf16_f32 %0, %2, %3\n\tv_cvt_pk_bf16_f32 %1, %4, %5" : "=&v"(p0[0]), "=&v"(p0[1]) : "v"(acc[0][0]), "v"(acc[0][1]), "v"(acc[0][2]), "v"(acc[0][3]));
  asm volatile("ds_write_b64 %0, %1\n\ts_nop 4" :: "v"(ad), "v"(p0) : "memory");
  asm volatile("v_cvt_pk_bf16_f32 %0, %2, %3\n\tv_cvt_pk_bf16_f32 %1, %4, %5" : "=&v"(p1[0]), "=&v"(p1[1]) : "v"(acc[1][0]), "v"(acc[1][1]), "v"(acc[1][2]), "v"(acc[1][3]));
  asm volatile("ds_write_b64 %0, %1 offset:2048" :: "v"(ad), "v"(p1) : "memory");
}
__device__ __forceinline__ void out_st(u32x4 v, int so, u32* out) {
  __builtin_amdgcn_raw_buffer_store_b128(v, __builtin_amdgcn_make_buffer_rsrc((void*)out, 0, 2 * MB_, 0x00020000), so, 0, 16);
}
template <bool L1>
__device__ __forceinline__ void wo_epi(const f32x4 (&acc)[2], const WoPre& P, char* smem, rsrc_t wrs, char* ws, u32* out, u32 ONES) {
  if (L1) {
    u32 v[4], t[8], lo, hi;
    asm volatile(
        "v_cvt_pk_bf16_f32 %4, %8, %9\n\t"
        "v_cvt_pk_bf16_f32 %5, %10, %11\n\t"
        "v_cvt_pk_bf16_f32 %6, %12, %13\n\t"
        "v_cvt_pk_bf16_f32 %7, %14, %15\n\t"
        "s_mov_b64 vcc, %[m0]\n\t"
        "v_cndmask_b32_dpp %1, %4, %6, vcc quad_perm:[1,0,3,2] row_mask:0xf bank_mask:0xf\n\t"
        "v_cndmask_b32_dpp %3, %5, %7, vcc quad_perm:[1,0,3,2] row_mask:0xf bank_mask:0xf\n\t"
        "s_not_b64 vcc, vcc\n\t"
        "v_cndmask_b32_dpp %4, %6, %4, vcc quad_perm:[1,0,3,2] row_mask:0xf bank_mask:0xf\n\t"
        "v_cndmask_b32_dpp %5, %7, %5, vcc quad_perm:[1,0,3,2] row_mask:0xf bank_mask:0xf\n\t"
        "v_perm_b32 %0, %1, %4, %[sl]\n\t"
        "v_perm_b32 %1, %1, %4, %[sh]\n\t"
        "v_perm_b32 %2, %3, %5, %[sl]\n\t"
        "v_perm_b32 %3, %3, %5, %[sh]\n\t"
        : "=&v"(v[0]), "=&v"(v[1]), "=&v"(v[2]), "=&v"(v[3]), "=&v"(t[0]), "=&v"(t[1]), "=&v"(t[2]), "=&v"(t[3])
        : "v"(acc[0][0]), "v"(acc[0][1]), "v"(acc[0][2]), "v"(acc[0][3]), "v"(acc[1][0]), "v"(acc[1][1]), "v"(acc[1][2]), "v"(acc[1][3]),
          [m0] "s"(P.m[0]), [sl] "s"(P.c[0]), [sh] "s"(P.c[1]) : "vcc", "scc");
    stw(wrs, P.so, (u32x4){v[0], v[1], v[2], v[3]});
    asm volatile(
        "s_mov_b64 vcc, %[m1]\n\t"
        "v_cndmask_b32 %0, %[v0], %[on], vcc\n\t"
        "v_cndmask_b32 %1, %[v1], %[on], vcc\n\t"
        "v_cndmask_b32 %2, %[v2], %[on], vcc\n\t"
        "v_cndmask_b32 %3, %[v3], %[on], vcc\n\t"
        "v_dot2_f32_bf16 %0, %[v0], %0, 0\n\t"
        "v_dot2_f32_bf16 %1, %[v1], %1, 0\n\t"
        "v_dot2_f32_bf16 %2, %[v2], %2, 0\n\t"
        "v_dot2_f32_bf16 %3, %[v3], %3, 0\n\t"
        "v_cndmask_b32_dpp %4, %[v0], %[on], vcc quad_perm:[2,3,0,1] row_mask:0xf bank_mask:0xf\n\t"
        "v_cndmask_b32_dpp %5, %[v1], %[on], vcc quad_perm:[2,3,0,1] row_mask:0xf bank_mask:0xf\n\t"
        "v_cndmask_b32_dpp %6, %[v2], %[on], vcc quad_perm:[2,3,0,1] row_mask:0xf bank_mask:0xf\n\t"
        "v_cndmask_b32_dpp %7, %[v3], %[on], vcc quad_perm:[2,3,0,1] row_mask:0xf bank_mask:0xf\n\t"
        "v_dot2c_f32_bf16_dpp %0, %[v0], %4 quad_perm:[2,3,0,1] row_mask:0xf bank_mask:0xf\n\t"
        "v_dot2c_f32_bf16_dpp %2, %[v2], %6 quad_perm:[2,3,0,1] row_mask:0xf bank_mask:0xf\n\t"
        "v_dot2c_f32_bf16_dpp %1, %[v1], %5 quad_perm:[2,3,0,1] row_mask:0xf bank_mask:0xf\n\t"
        "v_dot2c_f32_bf16_dpp %3, %[v3], %7 quad_perm:[2,3,0,1] row_mask:0xf bank_mask:0xf\n\t"
        "s_nop 0\n\t"
        "v_add_f32_dpp %4, %0, %0 row_shl:4 row_mask:0xf bank_mask:0x5\n\t"
        "v_add_f32_dpp %5, %2, %2 row_shl:4 row_mask:0xf bank_mask:0x5\n\t"
        "v_add_f32_dpp %4, %1, %1 row_shr:4 row_mask:0xf bank_mask:0xa\n\t"
        "v_add_f32_dpp %5, %3, %3 row_shr:4 row_mask:0xf bank_mask:0xa\n\t"
        "s_nop 0\n\t"
        "v_add_f32_dpp %0, %4, %4 row_ror:8 row_mask:0xf bank_mask:0x3\n\t"
        "v_add_f32_dpp %0, %5, %5 row_ror:8 row_mask:0xf bank_mask:0xc\n\t"
        "v_fract_f32 %1, %0\n\t"
        "v_cvt_flr_i32_f32 %[hi], %0\n\t"
        "v_mul_f32 %1, 0x4f800000, %1\n\t"
        "v_cvt_u32_f32 %1, %1\n\t"
        "v_and_or_b32 %[lo], %1, %[cm], 1\n\t"
        : "=&v"(t[0]), "=&v"(t[1]), "=&v"(t[2]), "=&v"(t[3]), "=&v"(t[4]), "=&v"(t[5]), "=&v"(t[6]), "=&v"(t[7]), [lo] "=&v"(lo), [hi] "=&v"(hi)
        : [v0] "v"(v[0]), [v1] "v"(v[1]), [v2] "v"(v[2]), [v3] "v"(v[3]), [on] "v"(ONES), [m1] "s"(P.m[1]), [cm] "s"(P.c[2]) : "vcc", "memory");
    const u64 mine = ((u64)hi << 32) | lo;
    const u64 old = __hip_atomic_fetch_add((LAS u64*)(size_t)P.xs, mine, __ATOMIC_RELAXED, __HIP_MEMORY_SCOPE_WORKGROUP);
    asm volatile("v_and_b32 %0, 3, %1\n\tv_cmpx_eq_u32 3, %0\n\tglobal_atomic_add_x2 %2, %3, %4\n\ts_mov_b64 exec, 1\n\tds_inc_u32 %5, %5\n\ts_mov_b64 exec, -1"
                 : "=&v"(t[0]) : "v"((u32)old), "v"(P.ao), "v"(old + mine), "s"(ws), "v"(L_MI + C_X2) : "vcc", "memory");
  }
}
template <bool L1>
__device__ __forceinline__ void res_ld(u32x2 (&res)[2], const char* smem, int tile, int hb, int a, int lane) {
#pragma unroll
  for (int t = 0; t < 2; ++t) {
    const int o = tile + t * 16384 + (64 * hb + 16 * a) * 32;
    if (L1) res[t] = *(const u32x2*)(smem + o + (lane & 14) * 32 + ((lane & 1) - t) * 16384 + ((lane & 1) ^ t) * 32 + (lane >> 4) * 8);
    else res[t] = __builtin_bit_cast(u32x2, __builtin_amdgcn_ds_read_tr16_b64_v4i16((LAS bf16x4*)(size_t)(u32)(o + lane * 8)));
  }
}
template <bool L1>
__device__ __forceinline__ void a_blayer(char* smem, rsrc_t wrs, char* ws, u32* out, int t, int a, bf16x8 (&wof)[16], u32 tag, u32x4 fl, u32x4 wfl, u32 g2, CPre& C) {
  t = (int)opaque((u32)t);
  int lane = t & 63;
  const int XIN = L1 ? OFF_X1 : OFF_X3, OBUF = L1 ? OFF_O1 : OFF_O2;
  const int bid = BIDX;
  const int rb = (bid & 7) * 4 + (bid >> 6), hb = (bid >> 3) & 7;
  const u32 ONES = opaque(0x3F803F80u);
  XPre xp;
  const int og = OBUF + rb * 32768;
  u32x2 resa[2], resb[2];
  const int wfo = OFF_ST0 + 8192 + (hb * 4 + a) * 128 + (lane & 3) * 16;
#define LDWO(e_) wof[e_] = __builtin_bit_cast(bf16x8, ldws(wrs, ((hb * 64 + a) * 64 + lane) * 16, OFF_WO + ((4 * a + (e_)) & 15) * 4096))
  auto ld_wo = [&](int e0, int e1) __attribute__((always_inline)) {
#pragma unroll
    for (int e = e0; e < e1; ++e) LDWO(e);
    __builtin_amdgcn_sched_barrier(0);
  };
  if (L1) {
    const int fo = OFF_ST0 + t * 16;
    if (SLP_XA1) __builtin_amdgcn_s_sleep(SLP_XA1);
    asm volatile("" : "+v"(fl), "+v"(wfl));
    tags_wait<true>(fl, wrs, fo, tag, 19);
    if ((u32)__builtin_amdgcn_readfirstlane(g2) < 4u) cnt_spin(smem, C_G2, 4);
    x_dma(wrs, XIN, 2 * rb, L_TA, a, lane);
    cnt_add(smem, C_S, lane);
    PAGEV(3);
    tags_wait(wfl, wrs, wfo, tag, 6);
  }
  if (L1) asm volatile("" : "+v"(lane));
  const int oo = (int)opaque((u32)(OBUF + ((rb * 32 + (hb * 2 + (a >> 1)) * 2 + ((lane >> 4) & 1)) * 64 + (2 * (a & 1) + (lane >> 5)) * 16 + (lane & 15)) * 16));
  const u32 sent = L1 ? x0_mark(tag) : 0xFFFFFFFFu;
  if (L1 && WOE) {
    ld_wo(0, WOE);
  }
  if (L1) { x_pre(xp, 0, a, lane, true); x_take<0, WOE, true>(smem, wrs, XIN, 2 * rb, a, lane, xp, sent); }
  if (!L1) { PAGE(6); VISIT(11); }
  ST(!L1, 10);
  lds_barrier();
  ST(!L1, 11);
  res_ld<L1>(resa, smem, L_TA, hb, a, lane);
  auto pf_x3 = [&]() __attribute__((always_inline)) {
    const u32x4 SENT = {0xFFFFFFFFu, 0xFFFFFFFFu, 0xFFFFFFFFu, 0xFFFFFFFFu};
    const int xo = OFF_X3 + (16 * a + (lane >> 2)) * XBS + ((bid & 7) * 32 + (bid >> 3)) * 64 + (lane & 3) * 16;
    stw(wrs, xo, SENT); stw(wrs, xo + 64 * XBS, SENT);
  };
  if (L1) pf_x3();
  if ((L1 ? SLP_XB1 : SLP_XB2) > 0) __builtin_amdgcn_s_sleep(L1 ? SLP_XB1 : SLP_XB2);
  ST(!L1, 12);
  x_dma(wrs, XIN, 64 + 2 * rb, L_TB, a, lane);
  const int ho = (int)opaque((u32)(KVO((rb - 1) & 31, !L1, 0, hb * 4 + a) + lane * 16));
  const u32 qo = opaque((u32)(L_QB + a * 3072 + lane * 16));
  x_pre(xp, 1, a, lane, L1);
  x_take<1, 0, L1>(smem, wrs, XIN, 64 + 2 * rb, a, lane, xp, sent);
  ST(!L1, 13);
  HaloMsg hm;
  if (L1) ld_wo(WOE, WO1);
  lds_barrier();
  ST(!L1, 14);
  if ((L1 ? SLP_H1 : SLP_H2) > 0) __builtin_amdgcn_s_sleep(L1 ? SLP_H1 : SLP_H2);
  halo_ld(hm, wrs, ho);
  {
    u32x4 r[3];
#pragma unroll
    for (int e = 0; e < 3; ++e) r[e] = *(const LAS u32x4*)(size_t)(qo + 1024 * e);
    attention(r, hm, ho, tag, wrs, oo, rb == 0, lane, xp.one, L1 ? 3 : 11);
  }
  ST(!L1, 16);
  if (L1) PAGEV(4);
  if (L1) ld_wo(WO1, 16);
  res_ld<L1>(resb, smem, L_TB, hb, a, lane);
  if (L1) { VISIT(5); VISIT(13); } else PAGE(7);
  __builtin_amdgcn_s_sleep(L1 ? SLP_OA1 : SLP_OA2);
  if (!L1 && SIG) cnt_wait<1>(smem, C_HR, 4);
  ST(!L1, 17);
  o_dma(wrs, og, L_TA, a, lane);
  WoPre wp;
  u32 qa[4];
#pragma unroll
  for (int e = 0; e < 4; ++e) qa[e] = opaque((u32)((L1 ? L_TA : L_O2) + ((a + e) & 3) * 8192 + lane * 16));
  wo_pre<L1>(wp, resa, ws, 0, rb, hb, a, lane);
  WoPre wq;
  if (!L1) wo_pre<L1>(wq, resb, ws, 1, rb, hb, a, lane);
  f32x4 acc[2];
  if (L1) __builtin_amdgcn_s_setprio(1);
  auto obq = [&]() __attribute__((always_inline)) {
    ST(!L1, 18);
    if (L1) wo_epi<L1>(acc, wp, smem, wrs, ws, out, ONES);
    if (L1) __builtin_amdgcn_s_setprio(0);
    if (L1) cnt_spin(smem, C_X2, 4);
    cnt_spin(smem, C_OB, L1 ? 4 : 8);
    o_dma(wrs, og + MB_, L1 ? L_TB : L_O2, a, lane);
  };
  wo_mma2<L1, !L1, L_TA>(acc, smem, wrs, og, a, lane, wof, L1 ? 4 : 12, wp.r, qa, sent, [&](int s) __attribute__((always_inline)) { if (s == 7) obq(); });
  if (L1) obq();
  if (L1) asm volatile("" : "+v"(lane));
  f32x4 aca[2] = {acc[0], acc[1]};
  const int soa = wp.so;
  const u32 xsa = wp.xs, aoa = wp.ao;
  u32x4 oa;
  if (L1) wo_pre<L1>(wp, resb, ws, 1, rb, hb, a, lane); else wp = wq;
  if (L1) c_pre(C, a, lane);
  if (L1) PAGEV(5);
  ST(!L1, 19);
  if (!L1) asm volatile("" : "+v"(aca[0]), "+v"(aca[1]));
  wo_mma2<L1, false, L1 ? L_TB : L_O2>(acc, smem, wrs, og + MB_, a, lane, wof, L1 ? 8 : 16, wp.r, qa, sent, [&](int s) __attribute__((always_inline)) {
    if (!L1 && s < 0) out_put(aca, aoa);
    if (!L1 && s == 8) oa = *(const LAS u32x4*)(size_t)xsa;
    if (!L1 && s == OSA) out_st(oa, soa, out);
    if (!L1 && s == OSA + 2) asm volatile("" :: "v"(oa));
  });
  if (!L1) {
    out_put(acc, wp.ao);
    lds_barrier();
    out_st(*(const LAS u32x4*)(size_t)wp.xs, wp.so, out);
  } else wo_epi<L1>(acc, wp, smem, wrs, ws, out, ONES);
  ST(!L1, 20); if (L1) ST(1, 1);
}

#undef DROLE_
#define DROLE_ 1
#define MMA(d_, a_, b_) asm volatile("v_mfma_f32_16x16x32_bf16 %0, %1, %2, %0" : "+v"(d_) : "v"(a_), "v"(b_))
__device__ __forceinline__ void mma_(f32x4& d, const bf16x8& a, const bf16x8& b) { MMA(d, a, b); }
template <int I, bool Z = false>
__device__ __forceinline__ void mma_w(f32x4& d, const bf16x8& w, const bf16x8& x) {
  if constexpr (I == 0 && !Z) asm volatile("v_mfma_f32_16x16x32_bf16 %0, %1, %2, %0" : "+v"(d) : "{v[76:79]}"(w), "v"(x));
  if constexpr (I == 0 && Z) asm volatile("v_mfma_f32_16x16x32_bf16 %0, %1, %2, 0" : "=&v"(d) : "{v[76:79]}"(w), "v"(x));
  if constexpr (I == 1 && Z) asm volatile("v_mfma_f32_16x16x32_bf16 %0, %1, %2, 0" : "=&v"(d) : "{v[80:83]}"(w), "v"(x));
  if constexpr (I == 2 && Z) asm volatile("v_mfma_f32_16x16x32_bf16 %0, %2, %1, 0" : "=&v"(d) : "{v[84:87]}"(w), "v"(x));
  if constexpr (I == 1 && !Z) asm volatile("v_mfma_f32_16x16x32_bf16 %0, %1, %2, %0" : "+v"(d) : "{v[80:83]}"(w), "v"(x));
  if constexpr (I == 2 && !Z) asm volatile("v_mfma_f32_16x16x32_bf16 %0, %2, %1, %0" : "+v"(d) : "{v[84:87]}"(w), "v"(x));
  if constexpr (I == 3) asm volatile("v_mfma_f32_16x16x32_bf16 %0, %1, %2, %0" : "+v"(d) : "{v[88:91]}"(w), "v"(x));
  if constexpr (I == 4) asm volatile("v_mfma_f32_16x16x32_bf16 %0, %1, %2, %0" : "+v"(d) : "{v[92:95]}"(w), "v"(x));
  if constexpr (I == 5) asm volatile("v_mfma_f32_16x16x32_bf16 %0, %2, %1, %0" : "+v"(d) : "{v[96:99]}"(w), "v"(x));
  if constexpr (I == 6) asm volatile("v_mfma_f32_16x16x32_bf16 %0, %1, %2, %0" : "+v"(d) : "{v[100:103]}"(w), "v"(x));
  if constexpr (I == 7) asm volatile("v_mfma_f32_16x16x32_bf16 %0, %1, %2, %0" : "+v"(d) : "{v[104:107]}"(w), "v"(x));
  if constexpr (I == 8) asm volatile("v_mfma_f32_16x16x32_bf16 %0, %2, %1, %0" : "+v"(d) : "{v[108:111]}"(w), "v"(x));
  if constexpr (I == 9) asm volatile("v_mfma_f32_16x16x32_bf16 %0, %1, %2, %0" : "+v"(d) : "{v[112:115]}"(w), "v"(x));
  if constexpr (I == 10) asm volatile("v_mfma_f32_16x16x32_bf16 %0, %1, %2, %0" : "+v"(d) : "{v[116:119]}"(w), "v"(x));
  if constexpr (I == 11) asm volatile("v_mfma_f32_16x16x32_bf16 %0, %2, %1, %0" : "+v"(d) : "{v[120:123]}"(w), "v"(x));
  if constexpr (I == 12) asm volatile("v_mfma_f32_16x16x32_bf16 %0, %1, %2, %0" : "+v"(d) : "{v[124:127]}"(w), "v"(x));
  if constexpr (I == 13) asm volatile("v_mfma_f32_16x16x32_bf16 %0, %1, %2, %0" : "+v"(d) : "{v[128:131]}"(w), "v"(x));
  if constexpr (I == 14) asm volatile("v_mfma_f32_16x16x32_bf16 %0, %2, %1, %0" : "+v"(d) : "{v[132:135]}"(w), "v"(x));
  if constexpr (I == 15) asm volatile("v_mfma_f32_16x16x32_bf16 %0, %1, %2, %0" : "+v"(d) : "{v[136:139]}"(w), "v"(x));
  if constexpr (I == 16) asm volatile("v_mfma_f32_16x16x32_bf16 %0, %1, %2, %0" : "+v"(d) : "{v[140:143]}"(w), "v"(x));
  if constexpr (I == 17) asm volatile("v_mfma_f32_16x16x32_bf16 %0, %2, %1, %0" : "+v"(d) : "{v[144:147]}"(w), "v"(x));
  if constexpr (I == 18) asm volatile("v_mfma_f32_16x16x32_bf16 %0, %1, %2, %0" : "+v"(d) : "{v[148:151]}"(w), "v"(x));
  if constexpr (I == 19) asm volatile("v_mfma_f32_16x16x32_bf16 %0, %1, %2, %0" : "+v"(d) : "{v[152:155]}"(w), "v"(x));
  if constexpr (I == 20) asm volatile("v_mfma_f32_16x16x32_bf16 %0, %2, %1, %0" : "+v"(d) : "{v[156:159]}"(w), "v"(x));
  if constexpr (I == 21) asm volatile("v_mfma_f32_16x16x32_bf16 %0, %1, %2, %0" : "+v"(d) : "{v[160:163]}"(w), "v"(x));
  if constexpr (I == 22) asm volatile("v_mfma_f32_16x16x32_bf16 %0, %1, %2, %0" : "+v"(d) : "{v[164:167]}"(w), "v"(x));
  if constexpr (I == 23) asm volatile("v_mfma_f32_16x16x32_bf16 %0, %2, %1, %0" : "+v"(d) : "{v[168:171]}"(w), "v"(x));
  if constexpr (I == 24) asm volatile("v_mfma_f32_16x16x32_bf16 %0, %1, %2, %0" : "+v"(d) : "{v[172:175]}"(w), "v"(x));
  if constexpr (I == 25) asm volatile("v_mfma_f32_16x16x32_bf16 %0, %1, %2, %0" : "+v"(d) : "{v[176:179]}"(w), "v"(x));
  if constexpr (I == 26) asm volatile("v_mfma_f32_16x16x32_bf16 %0, %2, %1, %0" : "+v"(d) : "{v[180:183]}"(w), "v"(x));
  if constexpr (I == 27) asm volatile("v_mfma_f32_16x16x32_bf16 %0, %1, %2, %0" : "+v"(d) : "{v[184:187]}"(w), "v"(x));
  if constexpr (I == 28) asm volatile("v_mfma_f32_16x16x32_bf16 %0, %1, %2, %0" : "+v"(d) : "{v[188:191]}"(w), "v"(x));
  if constexpr (I == 29) asm volatile("v_mfma_f32_16x16x32_bf16 %0, %2, %1, %0" : "+v"(d) : "{v[192:195]}"(w), "v"(x));
  if constexpr (I == 30) asm volatile("v_mfma_f32_16x16x32_bf16 %0, %1, %2, %0" : "+v"(d) : "{v[196:199]}"(w), "v"(x));
  if constexpr (I == 31) asm volatile("v_mfma_f32_16x16x32_bf16 %0, %1, %2, %0" : "+v"(d) : "{v[200:203]}"(w), "v"(x));
  if constexpr (I == 32) asm volatile("v_mfma_f32_16x16x32_bf16 %0, %2, %1, %0" : "+v"(d) : "{v[204:207]}"(w), "v"(x));
  if constexpr (I == 33) asm volatile("v_mfma_f32_16x16x32_bf16 %0, %1, %2, %0" : "+v"(d) : "{v[208:211]}"(w), "v"(x));
  if constexpr (I == 34) asm volatile("v_mfma_f32_16x16x32_bf16 %0, %1, %2, %0" : "+v"(d) : "{v[212:215]}"(w), "v"(x));
  if constexpr (I == 35) asm volatile("v_mfma_f32_16x16x32_bf16 %0, %2, %1, %0" : "+v"(d) : "{v[216:219]}"(w), "v"(x));
  if constexpr (I == 36) asm volatile("v_mfma_f32_16x16x32_bf16 %0, %1, %2, %0" : "+v"(d) : "{v[220:223]}"(w), "v"(x));
  if constexpr (I == 37) asm volatile("v_mfma_f32_16x16x32_bf16 %0, %1, %2, %0" : "+v"(d) : "{v[224:227]}"(w), "v"(x));
  if constexpr (I == 38) asm volatile("v_mfma_f32_16x16x32_bf16 %0, %2, %1, %0" : "+v"(d) : "{v[228:231]}"(w), "v"(x));
  if constexpr (I == 39) asm volatile("v_mfma_f32_16x16x32_bf16 %0, %1, %2, %0" : "+v"(d) : "{v[232:235]}"(w), "v"(x));
  if constexpr (I == 40) asm volatile("v_mfma_f32_16x16x32_bf16 %0, %1, %2, %0" : "+v"(d) : "{v[236:239]}"(w), "v"(x));
  if constexpr (I == 41) asm volatile("v_mfma_f32_16x16x32_bf16 %0, %2, %1, %0" : "+v"(d) : "{v[240:243]}"(w), "v"(x));
  if constexpr (I == 42) asm volatile("v_mfma_f32_16x16x32_bf16 %0, %1, %2, %0" : "+v"(d) : "{v[244:247]}"(w), "v"(x));
  if constexpr (I == 43) asm volatile("v_mfma_f32_16x16x32_bf16 %0, %1, %2, %0" : "+v"(d) : "{v[248:251]}"(w), "v"(x));
  if constexpr (I == 44) asm volatile("v_mfma_f32_16x16x32_bf16 %0, %2, %1, %0" : "+v"(d) : "{v[252:255]}"(w), "v"(x));
}
template <int I>
__device__ __forceinline__ void sum_w(f32x4& d, const u32x4& a, u32 ix, const bf16x8& w0, const bf16x8& w1) {
  if constexpr (I == 1) asm volatile("v_smfmac_f32_16x16x64_bf16 %0, %1, v[80:87], %2" : "+v"(d) : "v"(a), "v"(ix), "{v[80:83]}"(w0), "{v[84:87]}"(w1));
  if constexpr (I == 3) asm volatile("v_smfmac_f32_16x16x64_bf16 %0, %1, v[88:95], %2" : "+v"(d) : "v"(a), "v"(ix), "{v[88:91]}"(w0), "{v[92:95]}"(w1));
  if constexpr (I == 5) asm volatile("v_smfmac_f32_16x16x64_bf16 %0, %1, v[96:103], %2" : "+v"(d) : "v"(a), "v"(ix), "{v[96:99]}"(w0), "{v[100:103]}"(w1));
  if constexpr (I == 7) asm volatile("v_smfmac_f32_16x16x64_bf16 %0, %1, v[104:111], %2" : "+v"(d) : "v"(a), "v"(ix), "{v[104:107]}"(w0), "{v[108:111]}"(w1));
  if constexpr (I == 9) asm volatile("v_smfmac_f32_16x16x64_bf16 %0, %1, v[112:119], %2" : "+v"(d) : "v"(a), "v"(ix), "{v[112:115]}"(w0), "{v[116:119]}"(w1));
  if constexpr (I == 11) asm volatile("v_smfmac_f32_16x16x64_bf16 %0, %1, v[120:127], %2" : "+v"(d) : "v"(a), "v"(ix), "{v[120:123]}"(w0), "{v[124:127]}"(w1));
  if constexpr (I == 13) asm volatile("v_smfmac_f32_16x16x64_bf16 %0, %1, v[128:135], %2" : "+v"(d) : "v"(a), "v"(ix), "{v[128:131]}"(w0), "{v[132:135]}"(w1));
  if constexpr (I == 15) asm volatile("v_smfmac_f32_16x16x64_bf16 %0, %1, v[136:143], %2" : "+v"(d) : "v"(a), "v"(ix), "{v[136:139]}"(w0), "{v[140:143]}"(w1));
  if constexpr (I == 17) asm volatile("v_smfmac_f32_16x16x64_bf16 %0, %1, v[144:151], %2" : "+v"(d) : "v"(a), "v"(ix), "{v[144:147]}"(w0), "{v[148:151]}"(w1));
  if constexpr (I == 19) asm volatile("v_smfmac_f32_16x16x64_bf16 %0, %1, v[152:159], %2" : "+v"(d) : "v"(a), "v"(ix), "{v[152:155]}"(w0), "{v[156:159]}"(w1));
  if constexpr (I == 21) asm volatile("v_smfmac_f32_16x16x64_bf16 %0, %1, v[160:167], %2" : "+v"(d) : "v"(a), "v"(ix), "{v[160:163]}"(w0), "{v[164:167]}"(w1));
  if constexpr (I == 23) asm volatile("v_smfmac_f32_16x16x64_bf16 %0, %1, v[168:175], %2" : "+v"(d) : "v"(a), "v"(ix), "{v[168:171]}"(w0), "{v[172:175]}"(w1));
  if constexpr (I == 25) asm volatile("v_smfmac_f32_16x16x64_bf16 %0, %1, v[176:183], %2" : "+v"(d) : "v"(a), "v"(ix), "{v[176:179]}"(w0), "{v[180:183]}"(w1));
  if constexpr (I == 27) asm volatile("v_smfmac_f32_16x16x64_bf16 %0, %1, v[184:191], %2" : "+v"(d) : "v"(a), "v"(ix), "{v[184:187]}"(w0), "{v[188:191]}"(w1));
  if constexpr (I == 29) asm volatile("v_smfmac_f32_16x16x64_bf16 %0, %1, v[192:199], %2" : "+v"(d) : "v"(a), "v"(ix), "{v[192:195]}"(w0), "{v[196:199]}"(w1));
  if constexpr (I == 31) asm volatile("v_smfmac_f32_16x16x64_bf16 %0, %1, v[200:207], %2" : "+v"(d) : "v"(a), "v"(ix), "{v[200:203]}"(w0), "{v[204:207]}"(w1));
  if constexpr (I == 33) asm volatile("v_smfmac_f32_16x16x64_bf16 %0, %1, v[208:215], %2" : "+v"(d) : "v"(a), "v"(ix), "{v[208:211]}"(w0), "{v[212:215]}"(w1));
  if constexpr (I == 35) asm volatile("v_smfmac_f32_16x16x64_bf16 %0, %1, v[216:223], %2" : "+v"(d) : "v"(a), "v"(ix), "{v[216:219]}"(w0), "{v[220:223]}"(w1));
  if constexpr (I == 37) asm volatile("v_smfmac_f32_16x16x64_bf16 %0, %1, v[224:231], %2" : "+v"(d) : "v"(a), "v"(ix), "{v[224:227]}"(w0), "{v[228:231]}"(w1));
  if constexpr (I == 39) asm volatile("v_smfmac_f32_16x16x64_bf16 %0, %1, v[232:239], %2" : "+v"(d) : "v"(a), "v"(ix), "{v[232:235]}"(w0), "{v[236:239]}"(w1));
  if constexpr (I == 41) asm volatile("v_smfmac_f32_16x16x64_bf16 %0, %1, v[240:247], %2" : "+v"(d) : "v"(a), "v"(ix), "{v[240:243]}"(w0), "{v[244:247]}"(w1));
  if constexpr (I == 43) asm volatile("v_smfmac_f32_16x16x64_bf16 %0, %1, v[248:255], %2" : "+v"(d) : "v"(a), "v"(ix), "{v[248:251]}"(w0), "{v[252:255]}"(w1));
  if constexpr (I == 45) asm volatile("v_smfmac_f32_16x16x64_bf16 %0, %1, v[64:71], %2" : "+v"(d) : "v"(a), "v"(ix), "{v[64:67]}"(w0), "{v[68:71]}"(w1));
  if constexpr (I == 47) asm volatile("v_smfmac_f32_16x16x64_bf16 %0, %1, v[72:79], %2" : "+v"(d) : "v"(a), "v"(ix), "{v[72:75]}"(w0), "{v[76:79]}"(w1));
}
#define WREGS(X) \
  X(0, 76, 77, 78, 79) X(1, 80, 81, 82, 83) X(2, 84, 85, 86, 87) X(3, 88, 89, 90, 91) X(4, 92, 93, 94, 95) X(5, 96, 97, 98, 99) \
  X(6, 100, 101, 102, 103) X(7, 104, 105, 106, 107) X(8, 108, 109, 110, 111) X(9, 112, 113, 114, 115) X(10, 116, 117, 118, 119) X(11, 120, 121, 122, 123) \
  X(12, 124, 125, 126, 127) X(13, 128, 129, 130, 131) X(14, 132, 133, 134, 135) X(15, 136, 137, 138, 139) X(16, 140, 141, 142, 143) X(17, 144, 145, 146, 147) \
  X(18, 148, 149, 150, 151) X(19, 152, 153, 154, 155) X(20, 156, 157, 158, 159) X(21, 160, 161, 162, 163) X(22, 164, 165, 166, 167) X(23, 168, 169, 170, 171) \
  X(24, 172, 173, 174, 175) X(25, 176, 177, 178, 179) X(26, 180, 181, 182, 183) X(27, 184, 185, 186, 187) X(28, 188, 189, 190, 191) X(29, 192, 193, 194, 195) \
  X(30, 196, 197, 198, 199) X(31, 200, 201, 202, 203) X(32, 204, 205, 206, 207) X(33, 208, 209, 210, 211) X(34, 212, 213, 214, 215) X(35, 216, 217, 218, 219) \
  X(36, 220, 221, 222, 223) X(37, 224, 225, 226, 227) X(38, 228, 229, 230, 231) X(39, 232, 233, 234, 235) X(40, 236, 237, 238, 239) X(41, 240, 241, 242, 243) \
  X(42, 244, 245, 246, 247) X(43, 248, 249, 250, 251) X(44, 252, 253, 254, 255) X(45, 64, 65, 66, 67) X(46, 68, 69, 70, 71) X(47, 72, 73, 74, 75)
__device__ __forceinline__ void w_dma(rsrc_t r, int voff, int soff, int lds) {
  asm volatile("s_mov_b32 m0, %3\n\ts_nop 0\n\tbuffer_load_dwordx4 %0, %1, %2 offen lds\n\t"
               "s_add_u32 m0, m0, 3072\n\ts_nop 0\n\tbuffer_load_dwordx4 %0, %1, %2 offen offset:1024 lds\n\t"
               "s_add_u32 m0, m0, 3072\n\ts_nop 0\n\tbuffer_load_dwordx4 %0, %1, %2 offen offset:2048 lds"
               :: "v"(voff), "s"(r), "s"(soff), "s"(lds) : "memory", "scc");
}
template <int I>
__device__ __forceinline__ void w_ld(bf16x8& r, int vo, rsrc_t rs) {
#define X(n_, a_, b_, c_, d_) if constexpr (I == n_) asm volatile("buffer_load_dwordx4 %0, %1, %2, %3 offen offset:%4 sc1" : "={v[" #a_ ":" #d_ "]}"(r) : "v"(vo), "s"(rs), "s"(OFF_WQ + (I / 3) * 98304), "n"((I % 3) * 1024) : "memory");
  WREGS(X)
#undef X
}
template <int I, int O>
__device__ __forceinline__ void w_get(bf16x8& r, u32 a) {
#define X(n_, a_, b_, c_, d_) if constexpr (I == n_) asm volatile("ds_read_b64_tr_b16 v[" #a_ ":" #b_ "], %1 offset:%2\n\tds_read_b64_tr_b16 v[" #c_ ":" #d_ "], %1 offset:%3" : "=&{v[" #a_ ":" #d_ "]}"(r) : "v"(a), "n"(O), "n"(O + 2048) : "memory");
  WREGS(X)
#undef X
}
__device__ __forceinline__ float vmuln_(float a, float b) { float d; asm volatile("v_mul_f32_e64 %0, -%1, %2" : "=v"(d) : "v"(a), "v"(b)); return d; }
__device__ __forceinline__ float vmul_(float a, float b) { float d; asm volatile("v_mul_f32 %0, %1, %2" : "=v"(d) : "v"(a), "v"(b)); return d; }
__device__ __forceinline__ float vfman_(float c, float a, float b) { float d; asm volatile("v_fma_f32 %0, -%1, %2, %3" : "=v"(d) : "v"(a), "v"(b), "v"(c)); return d; }
__device__ __forceinline__ float vfma_(float a, float b, float c) { float d; asm volatile("v_fma_f32 %0, %1, %2, %3" : "=v"(d) : "v"(a), "v"(b), "v"(c)); return d; }
__device__ __forceinline__ u32 vcvt_(float a, float b) { u32 d; asm volatile("v_cvt_pk_bf16_f32 %0, %1, %2" : "=v"(d) : "v"(a), "v"(b)); return d; }
__device__ __forceinline__ float vsel0_(float a, u64 m) { float d; asm volatile("v_cndmask_b32_e64 %0, %1, 0, %2" : "=v"(d) : "v"(a), "s"(m)); return d; }
__device__ __forceinline__ u32 vlsa_(u32 a, int b) { u32 d; asm volatile("v_lshl_add_u32 %0, %1, 1, %2" : "=v"(d) : "v"(a), "s"(b)); return d; }
__device__ __forceinline__ float vmov_(u32 a) { float d; asm volatile("v_mov_b32 %0, %1" : "=v"(d) : "s"(a)); return d; }
__device__ __forceinline__ float vexp_(float a) { float d; asm volatile("v_exp_f32 %0, %1" : "=v"(d) : "v"(a)); return d; }
__device__ __forceinline__ float vrcp_(float a) { float d; asm volatile("v_rcp_f32 %0, %1" : "=v"(d) : "v"(a)); return d; }
__device__ __forceinline__ float vadd_(float a, float b) { float d; asm volatile("v_add_f32 %0, %1, %2" : "=v"(d) : "v"(a), "v"(b)); return d; }
__device__ __forceinline__ float vmax_(float a, float b) { float d; asm volatile("v_max_f32 %0, %1, %2" : "=v"(d) : "v"(a), "v"(b)); return d; }
__device__ __forceinline__ float vmax3_(float a, float b, float c) { float d; asm volatile("v_max3_f32 %0, %1, %2, %3" : "=v"(d) : "v"(a), "v"(b), "v"(c)); return d; }
__device__ __forceinline__ u32 vperm_(u32 a, u32 sl) { u32 d; asm volatile("v_perm_b32 %0, %1, %1, %2" : "=v"(d) : "v"(a), "v"(sl)); return d; }
__device__ __forceinline__ void vsw16_(float& a, float& b) { asm volatile("v_permlane16_swap_b32 %0, %1" : "+v"(a), "+v"(b)); }
__device__ __forceinline__ void vsw32_(float& a, float& b) { asm volatile("v_permlane32_swap_b32 %0, %1" : "+v"(a), "+v"(b)); }
__device__ __forceinline__ void mm16z_(f32x4& d, u32x2 a, u32x2 b) { asm volatile("v_mfma_f32_16x16x16_bf16 %0, %1, %2, 0" : "=&v"(d) : "v"(a), "v"(b)); }
#define NOP(n_) asm volatile("s_nop %0" :: "n"(n_))
__device__ __forceinline__ void touch_(bf16x4& a, bf16x4& b) { asm volatile("" : "+v"(a), "+v"(b)); }
__device__ __forceinline__ void vfmac_(float& d, float a, float b) { asm volatile("v_fmac_f32 %0, %1, %2" : "+v"(d) : "v"(a), "v"(b)); }
__device__ __forceinline__ u32 vadds_(u32 a, int b) { u32 d; asm volatile("v_add_u32 %0, %2, %1" : "=v"(d) : "v"(a), "s"(b)); return d; }
#define LD4(p_, o_) (*(const LAS f32x4*)((p_) + (o_)))
#define LD1(p_, o_) (*(const LAS float*)((p_) + (o_)))
#define LD2(p_, o_, d_) (f32x2){LD1(p_, o_), LD1(p_, (o_) + (d_))}
#define IN(o_, b_, c_) ((o_) >= (b_) && (o_) < (b_) + (c_))
struct GPre { u32 ap, fr, fq, fw, sp, l16, sl; };
struct GSum { f32x4 cs[2]; u32x4 on[3]; u32 ix; };
template <int I>
__device__ __forceinline__ void g_sum(GSum& S, bf16x8 (&wf)[48]) { sum_w<2 * I + 1>(S.cs[I & 1], S.on[(2 * I + 1) % 3], S.ix, wf[2 * I + 1], wf[(2 * I + 2) % 48]); }
__device__ __forceinline__ void g_pre(GPre& P, int g, bool pb) {
  const int lane = lane_id();
  P.ap = pb ? (L_TA + lane * 8 + (lane & 4) * 4096) ^ 32 : L_TA + lane * 8; P.fr = L_FS + g * 768 + (lane & 15) * 4; P.fq = L_FS + g * 768 + (lane >> 4) * 16; P.fw = L_FS + g * 768 + lane * 4;
  P.sp = L_SP + min(lane, 47) * 16; P.l16 = lane * 16;
  asm volatile("" : "+v"(P.ap), "+v"(P.fr), "+v"(P.fq), "+v"(P.fw), "+v"(P.sp), "+v"(P.l16));
}
#define SPIN(x_) asm volatile("" : "+s"(x_))
template <int LY, int H>
__device__ __forceinline__ void g_gemm(char* smem, rsrc_t wrs, int mo, int hso, int oo, u32 tag, int g, GPre& P, GSum& S, bf16x8 (&wf)[48]) {
  const LAS char *fr = (const LAS char*)(size_t)P.fr, *fq = (const LAS char*)(size_t)P.fq;
  constexpr int cso = 512;
  const LAS f32x4* sp = (const LAS f32x4*)(size_t)P.sp + 96 * H;
  if (H) PAGEV(10 + LY);
  if (LY || H) lds_barrier();
  else asm volatile("s_barrier" ::: "memory");
  ST(LY, 11 + 2 * H);
  u32x4 r[3], on;
  HaloMsg hm;
  GCst K;
  u32 su, ni, ao, u0, u1;
  bf16x4 xl[32], xh[32];
  bf16x8 wx[3];
  f32x4 aK[2], aV[2], aQ[2], s1, s2, cK, cQ, nv, rv;
  f32x2 rn;
  u32x4 mk, mv, q0, q1;
  float t0, t1, t2, t3, cV, tk[4], tv[4], tq[4];
#define OWS(s_) ((s_) < 16 ? 12 : OP2)
#define OKS(s_) (((s_) & 15) < OWS(s_) ? (s_) & 15 : ((s_) & 15) == OWS(s_) ? 15 : ((s_) & 15) - 1)
#define OLDL(s_) xl[s_] = __builtin_amdgcn_ds_read_tr16_b64_v4i16((LAS bf16x4*)((const LAS char*)(size_t)P.ap + (LY && (s_) < 16 ? 16384 : 0) + H * (L_TB - L_TA) + OKS(s_) * 1024))
#define OLDH(s_) xh[s_] = __builtin_amdgcn_ds_read_tr16_b64_v4i16((LAS bf16x4*)((const LAS char*)(size_t)P.ap + (LY && (s_) < 16 ? 16384 : 0) + H * (L_TB - L_TA) + OKS(s_) * 1024 + 512))
#pragma unroll
  for (int s = 0; s < 2; ++s) { OLDL(s); OLDH(s); }
  s1 = sp[0]; s2 = sp[48];
  SB();
#pragma unroll
  for (int s = 2; s < GPF; ++s) { OLDL(s); OLDH(s); }
  SB();
  const u32 qp = vadds_(P.l16, L_QB + g * 3072);
  const int ma = (int)vadds_(P.l16, mo);
  constexpr bool EX = LY ? EX2 : EX1;
  if (!EX) asm volatile("v_alignbit_b32 %0, %2, %2, %1" : "=v"(P.sl) : "v"(P.l16), "s"(0x0c0c0100u));
  if (H) {
    K.PAR = 0xAAAAAAAAAAAAAAAAull; K.VM[0] = 0x0fff00ff000f0000ull; K.VM[1] = 0x3fff03ff003f0003ull; K.VM[2] = 0xf000ff00fff0ffffull; K.VM[3] = 0xc000fc00ffc0fffcull; K.sc = 0x3eb8aa3bu;
    SPIN(K.PAR); SPIN(K.VM[0]); SPIN(K.VM[1]); SPIN(K.VM[2]); SPIN(K.VM[3]); SPIN(K.sc); SPIN(oo); SPIN(hso);
  }
  const float eps = vmov_(0x3727c5acu);
  auto slot = [&](auto N_) __attribute__((always_inline)) {
    constexpr int n = decltype(N_)::value, s = n / 3, j = n % 3, p = s >> 4, ks = OKS(s), m = n % 48;
    SB();
    if constexpr (j == 0 && !(s & 1)) touch_(xl[s + 1], xh[s + 1]);
    {
      const bf16x8 x = __builtin_shufflevector(xl[s], xh[s], 0, 1, 2, 3, 4, 5, 6, 7);
      if constexpr (ks == 15 && j == 0) mma_(aK[p], wx[0], x);
      else if constexpr (ks == 15 && j == 1) mma_(aV[p], x, wx[1]);
      else if constexpr (ks == 15 && j == 2) mma_(aQ[p], wx[2], x);
      else mma_w<3 * ks + (j + 1) % 3, ks == 0>(j == 0 ? aK[p] : j == 1 ? aV[p] : aQ[p], wf[3 * ks + (j + 1) % 3], x);
    }
    SB();
    if constexpr (j == 0 && s + GPF < 32) OLDL(s + GPF);
    if constexpr (j == 1 && s + GPF < 32) OLDH(s + GPF);
    SB();
    constexpr int wm = m - (3 * OWS(s) - 8);
    if constexpr (IN(wm, 0, 3)) wx[wm] = *(const LAS bf16x8*)(size_t)(qp + (L_WX - L_QB) + ((wm + 1) % 3) * 1024);
    if constexpr (n == 6) t0 = vadd_(s1[0], s1[1]);
    if constexpr (n == 7) t1 = vadd_(s1[2], s1[3]);
    if constexpr (n == 8) { t2 = vadd_(s2[0], s2[1]); t3 = vadd_(s2[2], s2[3]); }
    if constexpr (n == 9) t0 = vadd_(t0, t1);
    if constexpr (n == 10) t2 = vadd_(t2, t3);
    if constexpr (n == 11) { t0 *= (1.f / 512.f); PIN(t0); }
    if constexpr (n == 12) t1 = vfman_(eps, t0, t0);
    if constexpr (n == 13) { t1 = fmaf(t2, 1.f / 512.f, t1); PIN(t1); }
    if constexpr (n == 14) t1 = vmax_(t1, eps);
    if constexpr (n == 15) { t1 = __builtin_amdgcn_rsqf(t1); PIN(t1); }
    if constexpr (n == 16) t0 = vmuln_(t0, t1);
    if constexpr (n == 18) { LAS float* fw = (LAS float*)(size_t)P.fw; fw[0] = t1; fw[64] = t0; asm volatile("" ::: "memory"); }
    if constexpr (LY == 0 && H == 0) {
      if constexpr (n == 17) { asm volatile("s_waitcnt vmcnt(6)" ::: "memory"); g_sum<17>(S, wf); g_sum<18>(S, wf); }
      if constexpr (n == 20) { asm volatile("s_waitcnt vmcnt(3)" ::: "memory"); g_sum<19>(S, wf); }
      if constexpr (n == 23) { asm volatile("s_waitcnt vmcnt(0)" ::: "memory"); g_sum<20>(S, wf); g_sum<21>(S, wf); }
      if constexpr (n == 24) t2 = vadd_(S.cs[0][0], S.cs[0][1]);
      if constexpr (n == 25) { t3 = vadd_(S.cs[1][0], S.cs[1][1]); asm volatile("" :: "v"(S.cs[0]), "v"(S.cs[1])); }
      if constexpr (n == 26) t2 = vadd_(t2, t3);
      if constexpr (n == 27) { ((LAS float*)(size_t)P.fw)[128] = t2; asm volatile("" ::: "memory"); }
    }
    if constexpr (n == 31) cK = LD4(fq, cso + 64);
    if constexpr (n == 32) { rn = LD2(fr, 64, 256); cV = LD1(fr, cso + 128); }
    if constexpr (n == 33) nv = LD4(fq, 256 + 64);
    if constexpr (n == 34) rv = LD4(fq, 64);
    if constexpr (n == 37) mk[1] = __float_as_uint(vmov_(tag));
    if constexpr (n == 38) { tk[0] = vmul_(cK[0], rn[1]); tk[1] = vmul_(cK[1], rn[1]); }
    if constexpr (IN(n, 39, 2)) tk[n - 37] = vmul_(cK[n - 37], rn[1]);
    if constexpr (IN(n, 41, 4)) tv[n - 41] = vmul_(nv[n - 41], cV);
    if constexpr (n == 44) mk[3] = __float_as_uint(vmov_(tag));
    if constexpr (IN(n, 45, 2)) mv[2 * (n - 45) + 1] = __float_as_uint(vmov_(tag));
    if constexpr (n == 47) { vfmac_(tk[0], aK[0][0], rn[0]); vfmac_(tk[1], aK[0][1], rn[0]); vfmac_(tk[2], aK[0][2], rn[0]); }
    if constexpr (n == 48) { vfmac_(tk[3], aK[0][3], rn[0]); mk[0] = vcvt_(tk[0], tk[1]); vfmac_(tv[0], aV[0][0], rv[0]); }
    if constexpr (n == 49) { mk[2] = vcvt_(tk[2], tk[3]); vfmac_(tv[1], aV[0][1], rv[1]); stw(wrs, ma, mk); }
    if constexpr (n == 50) { vfmac_(tv[2], aV[0][2], rv[2]); vfmac_(tv[3], aV[0][3], rv[3]); mv[0] = vcvt_(tv[0], tv[1]); mv[2] = vcvt_(tv[2], tv[3]); }
    if constexpr (n == (LY ? KV2 : KV1)) stw(wrs, ma + 1024, mv);
    if constexpr (n == 53) cQ = LD4(fq, cso);
    if constexpr (n == KWN && !H) asm volatile("ds_write2_b32 %0, %1, %2 offset1:1" :: "v"(qp), "v"(mk[0]), "v"(mk[2]), "v"(mk) : "memory");
    if constexpr (n == KWN + 1 && !H) asm volatile("ds_write2_b32 %0, %1, %2 offset0:2 offset1:3" :: "v"(qp), "v"(mv[0]), "v"(mv[2]), "v"(mv) : "memory");
    if constexpr (IN(n, 59, 4)) tq[n - 59] = vmul_(cQ[n - 59], rn[1]);
    if constexpr (IN(n, 63, 4)) vfmac_(tq[n - 63], aQ[0][n - 63], rn[0]);
    if constexpr (IN(n, 67, 2)) q0[n - 67] = vcvt_(tq[2 * (n - 67)], tq[2 * (n - 67) + 1]);
    if constexpr (n == 69) rn = LD2(fr, 0, 256);
    if constexpr (n == 70) cK = LD4(fq, cso + 64);
    if constexpr (n == 72) nv = LD4(fq, 256);
    if constexpr (IN(n, 79, 4)) tq[n - 79] = vmul_(cQ[n - 79], rn[1]);
    if constexpr (n == 83) rv = LD4(fq, 0);
    if constexpr (IN(n, 84, 2)) { tk[2 * n - 168] = vmul_(cK[2 * n - 168], rn[1]); tk[2 * n - 167] = vmul_(cK[2 * n - 167], rn[1]); }
    if constexpr (IN(n, 86, 2)) { tv[2 * n - 172] = vmul_(nv[2 * n - 172], cV); tv[2 * n - 171] = vmul_(nv[2 * n - 171], cV); }
    static_assert(GPF == 4 && L_TA % 512 == 0, "");
    if constexpr (n == 35 && !LY) asm volatile("v_xor_b32 %0, 32, %0" : "+v"(P.ap));
    if constexpr (n == (LY ? PA2 : PA1)) __builtin_amdgcn_s_setprio(1);
    if constexpr (n == (LY ? PB2 : PB1)) __builtin_amdgcn_s_setprio(0);
    if constexpr (n == 90 && !LY && !H && EX) asm volatile("v_xor_b32 %0, 32, %0" : "+v"(P.ap));
    if constexpr (n == 90 && !EX) asm volatile("v_lshrrev_b32 %0, 1, %1" : "=v"(P.ap) : "v"(qp));
    if constexpr (n == 91 && !EX) P.ap = vadds_(P.ap, L_TB - (L_QB + g * 3072) / 2);
    if constexpr (H) {
      if constexpr (IN(n, 54, 2)) asm volatile("v_mov_b32 %0, %1" : "=v"(r[0][n - 54]) : "v"(mk[2 * (n - 54)]), "v"(mk));
      if constexpr (IN(n, 56, 2)) asm volatile("v_mov_b32 %0, %1" : "=v"(r[0][n - 54]) : "v"(mv[2 * (n - 56)]), "v"(mv));
      if constexpr (n == (LY ? NH2 : NH1)) { hm.k = ldws(wrs, P.l16, hso); hm.v = ldws(wrs, P.l16 + 1024, hso); }
      if constexpr (n == 89) { asm volatile("v_and_b32 %0, 0x100, %1" : "=v"(ao) : "v"(P.l16)); asm volatile("v_and_b32 %0, 0xf0, %1" : "=v"(u0) : "v"(P.l16)); }
      if constexpr (n == 90) { asm volatile("v_lshrrev_b32 %0, 9, %1" : "=v"(u1) : "v"(P.l16)); asm volatile("v_lshl_add_u32 %0, %0, 2, %1" : "+v"(ao) : "s"(oo)); }
      if constexpr (n == 91) { asm volatile("v_lshl_add_u32 %0, %1, 8, %0" : "+v"(u0) : "v"(u1)); asm volatile("v_alignbit_b32 %0, %2, %2, %1" : "=v"(P.sl) : "v"(P.l16), "s"(0x0c0c0100u)); }
      if constexpr (n == 92) { asm volatile("v_add_u32 %0, %0, %1" : "+v"(ao) : "v"(u0)); asm volatile("v_pk_max_i16 %0, %1, %2" : "=v"(su) : "v"(P.sl), "s"(0x03020302u)); }
      if constexpr (IN(n, 93, 2)) { asm volatile("v_mov_b32 %0, 0x3f803f80" : "=v"(on[2 * n - 186])); asm volatile("v_mov_b32 %0, 0x3f803f80" : "=v"(on[2 * n - 185])); }
      if constexpr (n == 95) asm volatile("v_mov_b32 %0, 0xff800000" : "=v"(ni));
    }
    if constexpr (n == 92) PIN(rv);
  };
  static_for<0, 96>(slot);
  SB();
#pragma unroll
  for (int e = 0; e < 4; ++e) vfmac_(tk[e], aK[1][e], rn[0]);
#pragma unroll
  for (int e = 0; e < 4; ++e) vfmac_(tv[e], aV[1][e], rv[e]);
  q0[2] = vcvt_(tk[0], tk[1]); q0[3] = vcvt_(tk[2], tk[3]);
  vfmac_(tq[0], aQ[1][0], rn[0]);
  if (!H) *(LAS u32x4*)(size_t)(qp + 1024) = q0;
  SB();
#pragma unroll
  for (int e = 1; e < 4; ++e) vfmac_(tq[e], aQ[1][e], rn[0]);
  q1[0] = vcvt_(tv[0], tv[1]); q1[1] = vcvt_(tv[2], tv[3]); q1[2] = vcvt_(tq[0], tq[1]); q1[3] = vcvt_(tq[2], tq[3]);
  if (!H) {
    *(LAS u32x4*)(size_t)(qp + 2048) = q1;
    asm volatile("" :: "v"(q0), "v"(q1) : "memory");
  } else {
  }
}
#define KPD 10
__device__ __forceinline__ void g_kv(char* smem, rsrc_t wrs, int mo, int hso, int oo, u32 tag, int g, GPre& P, bf16x8 (&wf)[48]) {
  const LAS char *fr = (const LAS char*)(size_t)P.fr, *fq = (const LAS char*)(size_t)P.fq;
  constexpr int cso = 512, W1 = 10, W2 = 5;
  const LAS f32x4* sp = (const LAS f32x4*)(size_t)P.sp + 96;
  PAGEV(11);
  lds_barrier();
  ST(1, 13);
  u32x4 r[3], on, mk, mv;
  HaloMsg hm;
  GCst K;
  u32 sl, su, ni, ao, u0, u1;
  bf16x4 xr[96];
  bf16x8 wx[3];
  f32x4 aK[2], aV[2], aQ[2], s1, s2, cK, cQ, nv, rv, tq0, tv;
  f32x2 rn, r0;
  float t0, t1, t2, t3, cV, tk[4], tq[4];
#define KKS(s_, w_) ((s_) < (w_) ? (s_) : (s_) == (w_) ? 15 : (s_) - 1)
#define KFO(f_) ((f_) < 16 ? 16384 + KKS(f_, W1) * 1024 : (((f_) & 1) ? 0 : 16384) + KKS(((f_) - 16) / 2, W2) * 1024)
#define KLD(r_) xr[r_] = __builtin_amdgcn_ds_read_tr16_b64_v4i16((LAS bf16x4*)((const LAS char*)(size_t)P.ap + (L_TB - L_TA) + KFO((r_) / 2) + ((r_) & 1) * 512))
#pragma unroll
  for (int e = 0; e < 4; ++e) KLD(e);
  s1 = sp[0]; s2 = sp[48];
  SB();
#pragma unroll
  for (int e = 4; e < KPD; ++e) KLD(e);
  SB();
  const u32 wp = vadds_(P.l16, L_WX + g * 3072);
  const int ma = (int)vadds_(P.l16, mo);
  K.PAR = 0xAAAAAAAAAAAAAAAAull; K.VM[0] = 0x0fff00ff000f0000ull; K.VM[1] = 0x3fff03ff003f0003ull; K.VM[2] = 0xf000ff00fff0ffffull; K.VM[3] = 0xc000fc00ffc0fffcull; K.sc = 0x3eb8aa3bu;
  u32 c0 = 0x0c0c0100u, c1 = 0x03020302u;
  SPIN(K.PAR); SPIN(K.VM[0]); SPIN(K.VM[1]); SPIN(K.VM[2]); SPIN(K.VM[3]); SPIN(K.sc); SPIN(oo); SPIN(hso); SPIN(c0); SPIN(c1);
  const float eps = vmov_(0x3727c5acu);
  mk[1] = __float_as_uint(vmov_(tag)); mk[3] = __float_as_uint(vmov_(tag)); mv[1] = __float_as_uint(vmov_(tag)); mv[3] = __float_as_uint(vmov_(tag));
  auto slot = [&](auto N_) __attribute__((always_inline)) {
    constexpr int n = decltype(N_)::value;
    constexpr int st = n < 32 ? n / 2 : n < 88 ? (n - 32) / 4 : 14 + (n < 92 ? (n - 88) / 2 : n & 1);
    constexpr int j = n < 32 ? 1 + (n & 1) : n < 88 ? n & 3 : n < 92 ? n & 1 : n < 94 ? 3 : 2;
    constexpr int f = n < 32 ? st : 16 + 2 * st + (j > 0), ks = KKS(st, n < 32 ? W1 : W2), p = n >= 32 && j, m = j % 3;
    SB();
    if constexpr (n % 4 == 0 && n < 92) touch_(xr[n + 2], xr[n + 3]);
    if constexpr (n == 90) touch_(xr[94], xr[95]);
    {
      const bf16x8 x = __builtin_shufflevector(xr[2 * f], xr[2 * f + 1], 0, 1, 2, 3, 4, 5, 6, 7);
      f32x4& d = m == 0 ? aQ[p] : m == 1 ? aK[p] : aV[p];
      if constexpr (ks == 15 && m == 2) mma_(d, x, wx[2]);
      else if constexpr (ks == 15) mma_(d, wx[m], x);
      else mma_w<3 * ks + m, ks == 0>(d, wf[3 * ks + m], x);
    }
    SB();
    if constexpr (n + KPD < 96) KLD(n + KPD);
    SB();
    if constexpr (IN(n, 13, 2)) wx[n - 12] = *(const LAS bf16x8*)(size_t)(wp + (n - 12) * 1024);
    if constexpr (IN(n, 44, 3)) wx[n - 44] = *(const LAS bf16x8*)(size_t)(wp + (n - 44) * 1024);
    if constexpr (n == 1) { t0 = vadd_(s1[0], s1[1]); t1 = vadd_(s1[2], s1[3]); }
    if constexpr (n == 2) { t2 = vadd_(s2[0], s2[1]); t3 = vadd_(s2[2], s2[3]); }
    if constexpr (n == 4) t0 = vadd_(t0, t1);
    if constexpr (n == 5) { t2 = vadd_(t2, t3); t0 *= (1.f / 512.f); PIN(t0); }
    if constexpr (n == 6) t1 = vfman_(eps, t0, t0);
    if constexpr (n == 7) { t1 = fmaf(t2, 1.f / 512.f, t1); PIN(t1); }
    if constexpr (n == 8) t1 = vmax_(t1, eps);
    if constexpr (n == 9) { t1 = __builtin_amdgcn_rsqf(t1); PIN(t1); }
    if constexpr (n == 10) t0 = vmuln_(t0, t1);
    if constexpr (n == 12) { LAS float* fw = (LAS float*)(size_t)P.fw; fw[0] = t1; fw[64] = t0; asm volatile("" ::: "memory"); }
    if constexpr (n == 16) cK = LD4(fq, cso + 64);
    if constexpr (n == 17) rn = LD2(fr, 64, 256);
    if constexpr (n == 18) cV = LD1(fr, cso + 128);
    if constexpr (n == 20) nv = LD4(fq, 256 + 64);
    if constexpr (n == 21) rv = LD4(fq, 64);
    if constexpr (IN(n, 24, 3)) tk[n - 24] = vmul_(cK[n - 24], rn[1]);
    if constexpr (n == 26) tk[3] = vmul_(cK[3], rn[1]);
    if constexpr (IN(n, 28, 3)) tv[n - 28] = vmul_(nv[n - 28], cV);
    if constexpr (n == 30) tv[3] = vmul_(nv[3], cV);
#define FMV(e_) do { float y_ = tv[e_]; vfmac_(y_, aV[0][e_], rv[e_]); tv[e_] = y_; } while (0)
    if constexpr (n == 32) { vfmac_(tk[0], aK[0][0], rn[0]); vfmac_(tk[1], aK[0][1], rn[0]); vfmac_(tk[2], aK[0][2], rn[0]); }
    if constexpr (n == 33) { vfmac_(tk[3], aK[0][3], rn[0]); mk[0] = vcvt_(tk[0], tk[1]); FMV(0); }
    if constexpr (n == 34) { mk[2] = vcvt_(tk[2], tk[3]); FMV(1); stw(wrs, ma, mk); }
    if constexpr (n == 35) { FMV(2); FMV(3); mv[0] = vcvt_(tv[0], tv[1]); mv[2] = vcvt_(tv[2], tv[3]); }
    if constexpr (n == 36) stw(wrs, ma + 1024, mv);
    if constexpr (IN(n, 37, 2)) asm volatile("v_mov_b32 %0, %1" : "=v"(r[0][n - 37]) : "v"(mk[2 * (n - 37)]), "v"(mk));
    if constexpr (IN(n, 40, 2)) asm volatile("v_mov_b32 %0, %1" : "=v"(r[0][n - 38]) : "v"(mv[2 * (n - 40)]), "v"(mv));
    if constexpr (n == 42) cQ = LD4(fq, cso);
    if constexpr (IN(n, 48, 3)) tq[n - 48] = vmul_(cQ[n - 48], rn[1]);
    if constexpr (n == 52) tq[3] = vmul_(cQ[3], rn[1]);
    if constexpr (n == 53) r0 = LD2(fr, 0, 256);
    if constexpr (n == 54) asm volatile("v_and_b32 %0, 0x100, %1" : "=v"(ao) : "v"(P.l16));
    if constexpr (n == 56) cK = LD4(fq, cso + 64);
    if constexpr (n == 57) nv = LD4(fq, 256);
    if constexpr (n == 58) asm volatile("v_lshl_add_u32 %0, %0, 2, %1" : "+v"(ao) : "s"(oo));
    constexpr int o = n >= 60 && n % 4 != 3 ? (n - 60) / 4 * 3 + n % 4 : -1;
    if constexpr (IN(o, 0, 4)) tq0[o] = vmul_(cQ[o], r0[1]);
    if constexpr (IN(o, 4, 4)) tk[o - 4] = vmul_(cK[o - 4], r0[1]);
    if constexpr (IN(o, 8, 4)) tv[o - 8] = vmul_(nv[o - 8], cV);
    if constexpr (o == 12) rv = LD4(fq, 0);
    if constexpr (o == 13) asm volatile("v_and_b32 %0, 0xf0, %1" : "=v"(u0) : "v"(P.l16));
    if constexpr (o == 14) asm volatile("v_lshrrev_b32 %0, 9, %1" : "=v"(u1) : "v"(P.l16));
    if constexpr (o == 15) asm volatile("v_lshl_add_u32 %0, %1, 8, %0" : "+v"(u0) : "v"(u1));
    if constexpr (o == 16) asm volatile("v_add_u32 %0, %0, %1" : "+v"(ao) : "v"(u0));
    if constexpr (o == 17) asm volatile("v_alignbit_b32 %0, %2, %2, %1" : "=v"(sl) : "v"(P.l16), "s"(c0));
    if constexpr (o == 18) asm volatile("v_pk_max_i16 %0, %1, %2" : "=v"(su) : "v"(sl), "s"(c1));
    if constexpr (o == 19) asm volatile("v_mov_b32 %0, 0xff800000" : "=v"(ni));
    if constexpr (IN(n, 90, 2)) { asm volatile("v_mov_b32 %0, 0x3f803f80" : "=v"(on[2 * n - 180])); asm volatile("v_mov_b32 %0, 0x3f803f80" : "=v"(on[2 * n - 179])); }
    if constexpr (n == NH2) { hm.k = ldws(wrs, P.l16, hso); hm.v = ldws(wrs, P.l16 + 1024, hso); }
    if constexpr (n == GPR || (n == 88 && GPE < 88)) __builtin_amdgcn_s_setprio(1);
    if constexpr (n == GPE) __builtin_amdgcn_s_setprio(0);
    if constexpr (n == NH2 + NSG && SIG == 1) cnt_add(smem, C_HR, 0);
    if constexpr (n == 92) { vfmac_(tq[0], aQ[0][0], rn[0]); vfmac_(tq[1], aQ[0][1], rn[0]); vfmac_(tq[2], aQ[0][2], rn[0]); }
    if constexpr (n == 93) { vfmac_(tq[3], aQ[0][3], rn[0]); r[1][0] = vcvt_(tq[0], tq[1]); vfmac_(tk[0], aK[1][0], r0[0]); }
    if constexpr (n == 94) { r[1][1] = vcvt_(tq[2], tq[3]); vfmac_(tk[1], aK[1][1], r0[0]); vfmac_(tk[2], aK[1][2], r0[0]); vfmac_(tk[3], aK[1][3], r0[0]); }
    if constexpr (n == 95) { r[1][2] = vcvt_(tk[0], tk[1]); r[1][3] = vcvt_(tk[2], tk[3]); }
  };
  static_for<0, 96>(slot);
  SB();
  ST(1, 14);
  attention_g(r, hm, (int)P.l16, hso, tag, wrs, (int)ao, sl, su, ni, on, K, 21, NH2 == 96, aQ[1], aV[1], tq0, tv, rv, r0[0]);
}
__device__ __forceinline__ void mmz_(f32x4& d, const u32x4& a, const u32x4& b) { asm volatile("v_mfma_f32_16x16x32_bf16 %0, %1, %2, 0" : "=&v"(d) : "v"(a), "v"(b)); }
constexpr int GN = 128, GW = 42;
constexpr int g_step(int n) { return n < 32 ? n / 2 : 16 + (n - 32) / 3; }
constexpr int g_pos(int n) { return n < 32 ? n & 1 : (n - 32) % 3; }
constexpr int g_rd(int n) { return g_pos(n) < 2 && g_step(n) + GPF < 48; }
constexpr int g_wt(int n) { return n < GN - 1 && g_pos(n + 1) == 0 && !(g_step(n + 1) & 1); }
struct GSched { int n, g[256], k[256], pos[256], first[GN + 2]; };
constexpr GSched g_sched() {
  GSched S{};
  int n = 0, used = 0;
  auto add = [&](int g, int k0, int k1, int mn = 0, int lat = 0, int w = 1, int each = 0) {
    for (int k = k0; k < k1; ++k) {
      int t = S.n ? S.pos[S.n - 1] + (k == k0 ? lat : each) : 0;
      if (t < mn) t = mn;
      if (t > n) { n = t; used = 0; }
      if (w >= 0) while (n < GN && used >= 2 - g_rd(n) - g_wt(n) + (n < 32 && (n & 3) >= GXH)) { ++n; used = 0; }
      S.g[S.n] = g; S.k[S.n] = k; S.pos[S.n++] = n;
      used = w >= 0 ? used + w : -g_rd(n);
    }
  };
  add(0, 0, 6, 1); add(0, 6, 10, 0, 1, 1, 1); add(0, 10, 11, 0, 1, 2); add(0, 11, 12, 0, 1); add(0, 12, 19);
  add(1, 0, 8, 0, GL1);
  add(2, 0, 4, 32); add(2, 4, 8, 35); add(2, 8, 54);
  add(3, 0, 2, 79); add(3, 2, 4, 80); add(3, 4, 5, 0, 0, -1); add(3, 5, 7); add(3, 7, 8, 0, 0, -1); add(3, 8, 12);
  add(4, 0, 9); add(4, 9, 11, 0, 1); add(5, 0, 1); add(4, 11, 12, 0, 1); add(5, 1, 2); add(4, 12, 14, 0, 1); add(5, 2, 3); add(4, 14, 15, 0, 1); add(5, 3, 4);
  add(4, 15, 16, 0, 1); add(4, 16, 17, 0, 1); add(4, 17, 21, 0, 1);
  add(4, 21, 25, 0, 0, 2); add(5, 4, 8); add(5, 15, 16); add(4, 25, 31); add(5, 8, 10); add(4, 31, 33, 0, 0, -1); add(5, 10, 15); add(4, 33, 34, 0, 1, 2); add(4, 34, 38, 0, 1); add(4, 38, 40, 0, 1);
  for (int i = 0, c = 0; i <= GN + 1; ++i) { while (c < S.n && S.pos[c] < i) ++c; S.first[i] = c; }
  return S;
}
constexpr GSched GSC = g_sched();
constexpr int g_at(int g, int k) { for (int i = 0; i < GSC.n; ++i) if (GSC.g[i] == g && GSC.k[i] == k) return GSC.pos[i]; return -1; }
static_assert(g_at(1, 3) < 31 && g_at(1, 7) < 32 && g_at(0, 18) < 26 && g_at(2, 53) < 79 && g_at(3, 5) >= 81 && g_at(4, 0) >= g_at(3, 7) + 2 && g_at(5, 3) + 5 < 32 + 3 * (GW - 16)
              && g_at(4, 33) >= g_at(4, 32) + 2 && g_at(4, 39) < GN, "schedule");
template <int LY>
__device__ __forceinline__ void g_att(char* smem, rsrc_t wrs, int oo, int g, const GPre& P, bf16x8 (&wf)[48]) {
  u32 ap = P.ap;
  const LAS char *fr = (const LAS char*)(size_t)P.fr, *fq = (const LAS char*)(size_t)P.fq;
  LAS float* fw = (LAS float*)(size_t)P.fw;
  constexpr int cso = 512, D = GPF;
  const LAS f32x4* sp = (const LAS f32x4*)(size_t)P.sp + 96;
  const u32 sl = P.sl;
  u32 su;
  u64 PAR = 0xAAAAAAAAAAAAAAAAull, VM[4] = {0x0fff00ff000f0000ull, 0x3fff03ff003f0003ull, 0xf000ff00fff0ffffull, 0xc000fc00ffc0fffcull};
  u32 cn = 0xff800000u, co = 0x3F803F80u, cu = 0x03020302u;
  int wo_ = L_WX + g * 3072 - 2 * L_TB;
  PAGEV(10 + LY);
  lds_barrier();
  ST(LY, 13);
  bf16x4 xl[48], xh[48];
  bf16x8 wk, wv, wq;
  f32x4 aK[3], aV[3], aQ[2], s1, s2, cK, cQ, m4, rv, S0, S1, oa;
  f32x2 rk[3], rq, mr;
  float t0, t1, t2, t3, mh, cV, tk[4], tv[4], b[4], l[4];
  u32 u0, wp, pw[2], ow[4], ao;
  u32x2 fk[3], fq0;
  u32x4 fv, pp, on;
#define GTO(s_) ((s_) < 16 ? L_TH - L_TB : (s_) < 32 || !LY ? 0 : 16384)
#define GKS(s_) ((s_) < 16 ? (s_) : (s_) < 32 ? ((s_) - 1) & 15 : (s_) == GW ? 15 : (s_) - 32 - ((s_) > GW))
#define LDL(s_) xl[s_] = __builtin_amdgcn_ds_read_tr16_b64_v4i16((LAS bf16x4*)((const LAS char*)(size_t)ap + GTO(s_) + GKS(s_) * 1024))
#define LDH(s_) xh[s_] = __builtin_amdgcn_ds_read_tr16_b64_v4i16((LAS bf16x4*)((const LAS char*)(size_t)ap + GTO(s_) + GKS(s_) * 1024 + 512))
#define WPTR() wp = vlsa_(ap, wo_)
#define LDW(e_) (*(const LAS bf16x8*)((const LAS char*)(size_t)wp + (e_) * 1024))
#pragma unroll
  for (int s = 0; s < 2; ++s) { LDL(s); LDH(s); }
  s1 = sp[0]; s2 = sp[48];
  SB();
#pragma unroll
  for (int s = 2; s < D; ++s) { LDL(s); LDH(s); }
  SB();
  SPIN(PAR); SPIN(VM[0]); SPIN(VM[1]); SPIN(VM[2]); SPIN(VM[3]); SPIN(cn); SPIN(co); SPIN(cu); SPIN(wo_); SPIN(oo);
  const float eps = vmov_(0x3727c5acu);
  auto op = [&](auto I_) __attribute__((always_inline)) {
    constexpr int p = GSC.g[decltype(I_)::value], o = GSC.k[decltype(I_)::value];
    if constexpr (p == 0) {
      if constexpr (o == 0) t0 = vadd_(s1[0], s1[1]);
      if constexpr (o == 1) t1 = vadd_(s1[2], s1[3]);
      if constexpr (o == 2) t2 = vadd_(s2[0], s2[1]);
      if constexpr (o == 3) t3 = vadd_(s2[2], s2[3]);
      if constexpr (o == 4) t0 = vadd_(t0, t1);
      if constexpr (o == 5) t2 = vadd_(t2, t3);
      if constexpr (o == 6) { t0 *= (1.f / 512.f); PIN(t0); }
      if constexpr (o == 7) t1 = vfman_(eps, t0, t0);
      if constexpr (o == 8) { t1 = fmaf(t2, 1.f / 512.f, t1); PIN(t1); }
      if constexpr (o == 9) t1 = vmax_(t1, eps);
      if constexpr (o == 10) { t1 = __builtin_amdgcn_rsqf(t1); PIN(t1); }
      if constexpr (o == 11) { fw[0] = t1; fw[64] = t0; asm volatile("" ::: "memory"); }
      if constexpr (o == 12) mr = LD2(fr, 256, 64);
      if constexpr (o == 13) cQ = LD4(fq, cso);
      if constexpr (o == 14) cK = LD4(fq, cso + 64);
      if constexpr (o == 15) WPTR();
      if constexpr (o == 16) wk = LDW(1);
      if constexpr (o == 17) wv = LDW(2);
      if constexpr (o == 18) wq = LDW(0);
    }
    if constexpr (p == 1) {
      if constexpr (IN(o, 0, 4)) aQ[0][o] = vmuln_(mr[0], cQ[o]);
      if constexpr (IN(o, 4, 4)) aK[0][o - 4] = vmuln_(mr[0], cK[o - 4]);
    }
    if constexpr (p == 2) {
      if constexpr (o == 0) mh = LD1(fr, 256 + 128);
      if constexpr (o == 1) cV = LD1(fr, cso + 128);
      if constexpr (o == 2) m4 = LD4(fq, 256 + 128);
      if constexpr (o == 3) rv = LD4(fq, 128);
      if constexpr (IN(o, 4, 4)) aQ[1][o - 4] = vmuln_(mr[1], cQ[o - 4]);
      if constexpr (IN(o, 8, 4)) tk[o - 8] = vfman_(aK[2][o - 8], mh, cK[o - 8]);
      if constexpr (IN(o, 12, 4)) aV[2][o - 12] = vfman_(aV[2][o - 12], m4[o - 12], cV);
      if constexpr (o == 16) m4 = LD4(fq, 256 + 64);
      if constexpr (IN(o, 17, 2)) fk[2][o - 17] = vcvt_(tk[2 * (o - 17)], tk[2 * (o - 17) + 1]);
      if constexpr (IN(o, 19, 4)) tk[o - 19] = vmul_(aV[2][o - 19], rv[o - 19]);
      if constexpr (o == 23) rv = LD4(fq, 0);
      if constexpr (IN(o, 24, 2)) fv[o - 24] = vcvt_(tk[2 * (o - 24)], tk[2 * (o - 24) + 1]);
      if constexpr (IN(o, 26, 4)) aV[1][o - 26] = vmuln_(m4[o - 26], cV);
      if constexpr (o == 30) m4 = LD4(fq, 256);
      if constexpr (o == 31) rq = LD2(fr, 0, 64);
      if constexpr (IN(o, 32, 4)) aK[1][o - 32] = vmuln_(mr[1], cK[o - 32]);
      if constexpr (o == 36) asm volatile("v_and_or_b32 %0, %1, 4, %2" : "=v"(u0) : "v"(sl), "v"(fq));
      if constexpr (o == 37) t0 = vmov_(cn);
      if constexpr (o == 38) rk[2] = LD2((const LAS char*)(size_t)u0, 128, 8);
      if constexpr (o == 39) rk[0] = LD2((const LAS char*)(size_t)u0, 0, 8);
      if constexpr (IN(o, 40, 4)) tv[o - 40] = vmuln_(m4[o - 40], cV);
      if constexpr (IN(o, 44, 4)) b[o - 44] = vsel0_(t0, VM[o - 44]);
      if constexpr (IN(o, 48, 4)) tv[o - 48] = vmul_(tv[o - 48], rv[o - 48]);
      if constexpr (IN(o, 52, 2)) { rq[o - 52] *= 0.25f * 1.4426950408889634f; PIN(rq[o - 52]); }
    }
    if constexpr (p == 3) {
      if constexpr (IN(o, 0, 2)) fq0[o] = vcvt_(aQ[0][2 * o], aQ[0][2 * o + 1]);
      if constexpr (IN(o, 2, 2)) fk[0][o - 2] = vcvt_(aK[0][2 * o - 4], aK[0][2 * o - 3]);
      if constexpr (o == 4) mm16z_(S0, fk[2], fq0);
      if constexpr (IN(o, 5, 2)) vfmac_(tv[o - 5], aV[0][o - 5], rv[o - 5]);
      if constexpr (o == 7) mm16z_(S1, fk[0], fq0);
      if constexpr (IN(o, 8, 2)) vfmac_(tv[o - 6], aV[0][o - 6], rv[o - 6]);
      if constexpr (IN(o, 10, 2)) fv[o - 8] = vcvt_(tv[2 * (o - 10)], tv[2 * (o - 10) + 1]);
    }
    if constexpr (p == 4) {
      if constexpr (IN(o, 0, 2)) l[o] = vsel_(S0[2 * o], S0[2 * o + 1], PAR);
      if constexpr (IN(o, 2, 2)) l[o] = vsel_(S1[2 * o - 4], S1[2 * o - 3], PAR);
      if constexpr (IN(o, 4, 4)) l[o - 4] = vfma_(l[o - 4], rk[o < 6 ? 2 : 0][o & 1], b[o - 4]);
      if constexpr (o == 8) t0 = vmax_(l[0], l[1]);
      if constexpr (o == 9) t2 = vmax3_(t0, l[2], l[3]);
      if constexpr (o == 10) t3 = vmax3_(t0, l[2], l[3]);
      if constexpr (o == 11) vsw16_(t2, t3);
      if constexpr (o == 12) t0 = vmax_(t2, t3);
      if constexpr (o == 13) t1 = vmax_(t2, t3);
      if constexpr (o == 14) vsw32_(t0, t1);
      if constexpr (o == 15) t0 = vmax_(t0, t1);
      if constexpr (o == 16) t0 = vmuln_(t0, rq[0]);
      if constexpr (IN(o, 17, 4)) l[o - 17] = vfma_(l[o - 17], rq[0], t0);
      if constexpr (IN(o, 21, 4)) l[o - 21] = vexp_(l[o - 21]);
      if constexpr (IN(o, 25, 2)) pw[o - 25] = vcvt_(l[2 * (o - 25)], l[2 * (o - 25) + 1]);
      if constexpr (IN(o, 27, 4)) pp[o - 27] = vperm_(pw[(o - 27) >> 1], o & 1 ? sl : su);
      if constexpr (o == 31) mmz_(S0, on, pp);
      if constexpr (o == 32) mmz_(oa, fv, pp);
      if constexpr (o == 33) { t0 = vrcp_(S0[0]); asm volatile("" :: "v"(S0)); }
      if constexpr (IN(o, 34, 4)) tk[o - 34] = vmul_(oa[o - 34], t0);
      if constexpr (IN(o, 38, 2)) ow[o - 38] = vcvt_(tk[2 * (o - 38)], tk[2 * (o - 38) + 1]);
    }
    if constexpr (p == 5) {
      static_assert(L_TB % 512 == 0, "lane bits of ap");
      if constexpr (o == 0 && !LY) { asm volatile("v_bitop3_b32 %0, %1, 32, %2 bitop3:0x28" : "=v"(wp) : "v"(ap), "s"(0x1FF)); wp = vlsa_(wp, wo_ + 2 * L_TB); }
      if constexpr (o == 0 && LY) WPTR();
      if constexpr (o == 1) wq = LDW(0);
      if constexpr (o == 2) wk = LDW(1);
      if constexpr (o == 3) wv = LDW(2);
      if constexpr (IN(o, 4, 4)) on[o - 4] = __float_as_uint(vmov_(co));
      if constexpr (o == 8) rk[1] = LD2((const LAS char*)(size_t)u0, 64, 8);
      if constexpr (o == 9) rv = LD4(fq, 64);
      if constexpr (o == 10 && !LY) { static_assert(g_at(5, 10) > 32 + 3 * (47 - GPF - 16) + 1, "last read"); ap ^= 32u; PIN(ap); }
      if constexpr (o == 10) asm volatile("v_and_b32 %0, 0x80, %1" : "=v"(ao) : "v"(ap));
      if constexpr (o == 11) asm volatile("v_and_b32 %0, 0x78, %1" : "=v"(u0) : "v"(ap));
      if constexpr (o == 12) asm volatile("v_and_b32 %0, 0x178, %1" : "=v"(wp) : "v"(ap));
      if constexpr (o == 13) asm volatile("v_lshl_add_u32 %0, %0, 3, %1" : "+v"(ao) : "s"(oo));
      if constexpr (o == 15) asm volatile("v_pk_max_i16 %0, %1, %2" : "=v"(su) : "v"(sl), "s"(cu));
      if constexpr (o == 14) asm volatile("v_add3_u32 %0, %0, %1, %2" : "+v"(ao) : "v"(u0), "v"(wp));
    }
  };
  auto slot = [&](auto N_) __attribute__((always_inline)) {
    constexpr int n = decltype(N_)::value, s = g_step(n), j = g_pos(n), ks = GKS(s);
    constexpr int m = n < 32 ? j + 1 : j;
    constexpr int t = n < 32 ? 2 : n < 80 ? 0 : 1;
    SB();
    if constexpr (j == 0 && !(s & 1)) touch_(xl[s + 1], xh[s + 1]);
    {
      const bf16x8 x = __builtin_shufflevector(xl[s], xh[s], 0, 1, 2, 3, 4, 5, 6, 7);
      if constexpr (ks == 15 && m == 1) mma_(aK[t], wk, x);
      else if constexpr (n == 34) mmz_(aV[t], __builtin_bit_cast(u32x4, x), __builtin_bit_cast(u32x4, wv));
      else if constexpr (ks == 15 && m == 2) mma_(aV[t], x, wv);
      else if constexpr (ks == 15 && m == 0) mma_(aQ[t & 1], wq, x);
      else mma_w<3 * ks + m, n < 2>(m == 1 ? aK[t] : m == 2 ? aV[t] : aQ[t & 1], wf[3 * ks + m], x);
    }
    SB();
    if constexpr (j == 0 && s + D < 48) LDL(s + D);
    if constexpr (j == 1 && s + D < 48) LDH(s + D);
    SB();
    static_for<GSC.first[n], GSC.first[n + 1]>(op);
    static_assert(g_at(0, 15) < 22, "");
    if constexpr (!LY && n == 23) { u32& a_ = ap; a_ += (a_ & 32u) << 9; PIN(a_); }
    if constexpr (!LY && n == 32 + 3 * (32 - GPF - 16) - 1) { u32& a_ = ap; a_ ^= 32u; PIN(a_); }
    if constexpr (n == GN - 2) { f32x4& r_ = rv; PIN(r_); }
  };
  static_for<0, GN>(slot);
  SB();
  ST(LY, 14);
  asm volatile(
      "v_cvt_pk_bf16_f32 v8, v8, v9\n\t"
      "v_cvt_pk_bf16_f32 v9, v10, v11\n\t"
      "s_nop 1\n\t"
      "v_mfma_f32_16x16x16_bf16 v[20:23], %[fk0], v[8:9], 0\n\t"
      "v_cvt_pk_bf16_f32 v4, v4, v5\n\t"
      "v_cvt_pk_bf16_f32 v5, v6, v7\n\t"
      "v_mul_f32 v12, v12, v16\n\t"
      "v_mul_f32 v13, v13, v17\n\t"
      "v_mfma_f32_16x16x16_bf16 v[24:27], v[4:5], v[8:9], 0\n\t"
      "v_mul_f32 v14, v14, v18\n\t"
      "v_mul_f32 v15, v15, v19\n\t"
      "v_cvt_pk_bf16_f32 v32, v12, v13\n\t"
      "v_cndmask_b32_e64 v20, v20, v21, %[par]\n\t"
      "v_cndmask_b32_e64 v21, v22, v23, %[par]\n\t"
      "v_fma_f32 v20, v20, %[r00], %[b0]\n\t"
      "v_fma_f32 v21, v21, %[r01], %[b1]\n\t"
      "v_max_f32 v10, v20, v21\n\t"
      "v_cndmask_b32_e64 v22, v24, v25, %[par]\n\t"
      "v_cndmask_b32_e64 v23, v26, v27, %[par]\n\t"
      "v_fma_f32 v22, v22, %[r10], %[b2]\n\t"
      "v_fma_f32 v23, v23, %[r11], %[b3]\n\t"
      "v_max3_f32 v25, v10, v22, v23\n\t"
      "v_max3_f32 v24, v10, v22, v23\n\t"
      "v_cvt_pk_bf16_f32 v33, v14, v15\n\t"
      "s_nop 0\n\t"
      "v_permlane16_swap_b32 v24, v25\n\t"
      "v_max_f32 v26, v24, v25\n\t"
      "v_max_f32 v27, v24, v25\n\t"
      "s_nop 1\n\t"
      "v_permlane32_swap_b32 v26, v27\n\t"
      "v_max_f32 v24, v26, v27\n\t"
      "v_mul_f32_e64 v24, -v24, %[sc]\n\t"
      "v_fma_f32 v20, v20, %[sc], v24\n\t"
      "v_fma_f32 v21, v21, %[sc], v24\n\t"
      "v_exp_f32 v20, v20\n\t"
      "v_exp_f32 v21, v21\n\t"
      "v_fma_f32 v22, v22, %[sc], v24\n\t"
      "v_fma_f32 v23, v23, %[sc], v24\n\t"
      "v_exp_f32 v22, v22\n\t"
      "v_exp_f32 v23, v23\n\t"
      "v_cvt_pk_bf16_f32 v24, v20, v21\n\t"
      "v_cvt_pk_bf16_f32 v26, v22, v23\n\t"
      "v_perm_b32 v6, v24, v24, %[sl]\n\t"
      "v_perm_b32 v7, v24, v24, %[su]\n\t"
      "v_perm_b32 v4, v26, v26, %[sl]\n\t"
      "v_perm_b32 v5, v26, v26, %[su]\n\t"
      "s_nop 1\n\t"
      "v_mfma_f32_16x16x32_bf16 v[20:23], %[on], v[4:7], 0\n\t"
      "v_mfma_f32_16x16x32_bf16 v[8:11], v[32:35], v[4:7], 0\n\t"
      "s_nop 3\n\t"
      "v_rcp_f32 v24, v20\n\t"
      "s_nop 2\n\t"
      "v_mul_f32 v8, v8, v24\n\t"
      "v_mul_f32 v9, v9, v24\n\t"
      "v_mul_f32 v10, v10, v24\n\t"
      "v_mul_f32 v11, v11, v24\n\t"
      "v_cvt_pk_bf16_f32 v30, v8, v9\n\t"
      "v_cvt_pk_bf16_f32 v31, v10, v11\n\t"
      "s_nop 0\n\t"
      "v_permlane16_swap_b32 v28, v30\n\t"
      "v_permlane16_swap_b32 v29, v31\n\t"
      : "+{v[4:7]}"(aK[1]), "+{v[8:11]}"(aQ[1]), "+{v[12:15]}"(aV[1]), "=&{v[20:23]}"(S0), "=&{v[24:27]}"(S1), "+{v28}"(ow[0]), "+{v29}"(ow[1]), "=&{v30}"(ow[2]), "=&{v31}"(ow[3]), "+{v[32:35]}"(fv)
      : "{v[16:19]}"(rv), [sc] "v"(rq[1]), [par] "s"(PAR), [fk0] "v"(fk[0]), [on] "v"(on), [r00] "v"(rk[0][0]), [r01] "v"(rk[0][1]), [r10] "v"(rk[1][0]), [r11] "v"(rk[1][1]),
        [b0] "v"(b[0]), [b1] "v"(b[1]), [b2] "v"(b[2]), [b3] "v"(b[3]), [sl] "v"(sl), [su] "v"(su));
  u32x4 ov = {ow[0], ow[1], ow[2], ow[3]};
  stw(wrs, (int)ao, ov);
  asm volatile("s_nop 1" :: "v"(ov) : "memory");
}
template <bool L1>
__device__ __forceinline__ void g_blayer(char* smem, rsrc_t wrs, int g, u32 tag, GPre& P, GSum& S, bf16x8 (&wf)[48]) {
  const int bid = BIDX;
  const int rb = (bid & 7) * 4 + (bid >> 6), hb = (bid >> 3) & 7;
  const int mo = KVO(rb, !L1, 0, hb * 4 + g);
  if (!L1) ST(1, 10);
  int oo = (L1 ? OFF_O1 : OFF_O2) + MB_ + ((rb * 32 + (hb * 2 + (g >> 1)) * 2) * 64 + 32 * (g & 1)) * 16;
  g_gemm<!L1, 0>(smem, wrs, mo, 0, 0, tag, g, P, S, wf);
  ST(!L1, 12);
  if constexpr (!(L1 ? EX1 : EX2)) g_att<!L1>(smem, wrs, oo, g, P, wf);
  else g_kv(smem, wrs, KVO(rb, !L1, 1, hb * 4 + g), KVO((rb - 1) & 31, !L1, rb != 0 && !FK, hb * 4 + g), oo, tag, g, P, wf);
  ST(!L1, 16);
  if (L1) PAGE(12);
  __builtin_amdgcn_s_sleep(L1 ? SLP_OB1 : SLP_OB2);
  cnt_add(smem, C_OB, 0);
}

using u32x8 = __attribute__((ext_vector_type(8))) unsigned;
using u32x16 = __attribute__((ext_vector_type(16))) unsigned;
__global__ __launch_bounds__(512, 2) KATTR void k_mega(const u16*, const u16*, const u16*, const u16*, const u16*, const u16*, const u16*, const u16*,
                                                       const u16*, const u16*, const u16*, char*, u32*) {
  extern __shared__ __attribute__((aligned(16))) char smem[];
  u32x16 ka;
  u32x8 kb;
  u32x2 kc;
  asm volatile("s_load_dwordx16 %0, %3, 0x0\n\ts_load_dwordx8 %1, %3, 0x40\n\ts_load_dwordx2 %2, %3, 0x60"
               : "=&s"(ka), "=&s"(kb), "=&s"(kc) : "s"(__builtin_amdgcn_kernarg_segment_ptr()) : "memory");
  const int tid = TIDX, lane = tid & 63;
  const int w = __builtin_amdgcn_readfirstlane(tid >> 6);
  const int bid = BIDX;
  ST_INIT(); ST(0, 0); ST(1, 0);
  if (w == 0) {
    u64 pc;
    int clen;
    asm volatile("s_getpc_b64 %0\n.Lcw_base:\n\ts_mov_b32 %1, .Lcw_end-.Lcw_base-4" : "=s"(pc), "=s"(clen));
    asm volatile("s_mov_b32 m0, %2\n\ts_nop 0\n\tglobal_load_lds_dword %0, %1" :: "v"(min(min(lane, 15) * 128 + (bid >> 3) * 2048, clen)), "s"(pc), "s"(L_FS) : "memory");
  }
  const int kp = (bid & 7) * 32 + (bid >> 3), hb = (bid >> 3) & 7;
  const int c = (w >> 1) & 1, g = w & 3;
  u32 po = (u32)(kp * 512 + (tid & 255) * 2);
  u32 xo = (u32)((w < 4 ? ((bid >> 3) * 64 + 16 * w) * 1024 + (bid & 7) * 128 : bid * 8192 + g * 2048) + (lane & 7) * 16 + (lane >> 3) * (w < 4 ? 2048 : 128));
  asm volatile("" : "+v"(po), "+v"(xo) :: "memory");
  asm volatile("s_waitcnt lgkmcnt(0)" : "+s"(ka), "+s"(kb), "+s"(kc) :: "memory");
#define KP(T_, v_, i_) ((T_)(((u64)v_[2 * (i_) + 1] << 32) | v_[2 * (i_)]))
  const u16 *x0 = KP(const u16*, ka, 0), *A_re = KP(const u16*, ka, 1), *A_im = KP(const u16*, ka, 2), *B_re = KP(const u16*, ka, 3), *B_im = KP(const u16*, ka, 4);
  const u16 *C_re = KP(const u16*, ka, 5), *C_im = KP(const u16*, ka, 6), *log_dt = KP(const u16*, ka, 7), *Dskip = KP(const u16*, kb, 0), *wqkv = KP(const u16*, kb, 1), *wo = KP(const u16*, kb, 2);
  char* ws = KP(char*, kb, 3);
  u32* out = KP(u32*, kc, 0);
  u32x4 fx[2], fp, fq;
  u32 ldtw, dskw;
#define LD_A "global_load_ushort v248, %[po], %[ar]\n\tglobal_load_ushort v249, %[po], %[ai]\n"
  asm volatile("s_cmp_gt_u32 %[w], 3\n\ts_cbranch_scc1 0f\n\ts_load_dword %[dt], %[pd], %[k4]\n\ts_load_dword %[ds], %[ps], %[k4]\n\t" LD_A
               "0:\n\tglobal_load_dwordx4 v[240:243], %[xo], %[x0]\n\tglobal_load_dwordx4 v[244:247], %[xo], %[x0] offset:1024\n\tglobal_load_dword v254, %[eo], %[ws] sc1\n\t"
               "s_cbranch_scc0 1f\n\t"
               "global_load_ushort v250, %[po], %[br]\n\tglobal_load_ushort v251, %[po], %[bi]\n\tglobal_load_ushort v252, %[po], %[cr]\n\tglobal_load_ushort v253, %[po], %[ci]\n\t" LD_A "1:"
               : "=&{v[240:243]}"(fx[0]), "=&{v[244:247]}"(fx[1]), "=&{v[248:251]}"(fp), "=&{v[252:255]}"(fq), [dt] "=&s"(ldtw), [ds] "=&s"(dskw)
               : [w] "s"(w), [po] "v"(po), [xo] "v"(xo), [eo] "v"(OFF_EP), [k4] "s"(4 * kp), [x0] "s"(x0), [ar] "s"(A_re), [ai] "s"(A_im), [br] "s"(B_re), [bi] "s"(B_im),
                 [cr] "s"(C_re), [ci] "s"(C_im), [pd] "s"(log_dt), [ps] "s"(Dskip), [ws] "s"(ws) : "memory", "scc");
  ST(2, 1);
  __builtin_amdgcn_sched_barrier(0);
  const rsrc_t wrs = __builtin_amdgcn_make_buffer_rsrc((void*)ws, 0, WSB, 0x00020000);
  if (w >= 4) {
    asm volatile("s_mov_b32 m0, %2\n\ts_nop 0\n\tglobal_load_lds_dword %0, %1" :: "v"((bid * 33 + min(g * 9 + min(lane, 8), 32)) * 128), "s"(wqkv), "s"(L_FS + w * 256) : "memory");
    u32x4 zz = {0, 0, 0, 0};
    asm volatile("" : "+v"(zz));
    const int z0 = (int)opaque((u32)(L_U + (lane >> 5) * 4608 + (lane & 31) * 16)), z1 = (int)opaque((u32)(L_MI + min((tid & 255) * 16 - 1024, 1088)));
    *(u32x4*)(smem + (g == 0 ? z0 : z1)) = zz;
    asm volatile("s_nop 1\n\ts_waitcnt lgkmcnt(0)" :: "v"(zz) : "memory");
  }
  __builtin_amdgcn_s_barrier();
  __builtin_amdgcn_sched_barrier(0);
  const int ftb = c ? L_TA + 6400 : 0;
#define PUT(e_, r_, i_) *(LAS u32*)(wa + (e_) * FTS) = pkh(r_, i_)
#define CMR(a_, b_) fmaf(a_[0], b_[0], -(a_[1] * b_[1]))
#define CMI(a_, b_) fmaf(a_[0], b_[1], a_[1] * b_[0])
#define CMUL(a_, b_) (f32x2){CMR(a_, b_), CMI(a_, b_)}
  struct P0A { u32 gad[12], sad[8], ua, ur; };
  const u32 sb = (u32)((L_KT + c * TBS - 0x7E00) & 0xFFFF) * 0x10001u;
  auto p0_gad = [&](P0A& A, int lane) __attribute__((always_inline)) {
    const float f0 = __uint_as_float(opaque(__float_as_uint((float)(1023 - 512 * (w & 1) - 8 * lane))));
#pragma unroll
    for (int e = 0; e < 6; ++e) { const u32 ga = tb_addr(pk2(f0 - (float)(2 * e), f0 - (float)(2 * e + 1)), sb); A.gad[2 * e] = ga & 0xFFFFu; A.gad[2 * e + 1] = ga >> 16; }
    asm volatile("" : "+v"(A.gad[0]), "+v"(A.gad[1]), "+v"(A.gad[2]), "+v"(A.gad[3]), "+v"(A.gad[4]), "+v"(A.gad[5]), "+v"(A.gad[6]), "+v"(A.gad[7]), "+v"(A.gad[8]), "+v"(A.gad[9]), "+v"(A.gad[10]));
    __builtin_amdgcn_sched_barrier(0);
  };
  auto p0_sad = [&](P0A& A, int lane) __attribute__((always_inline)) {
    lane = (int)opaque((u32)lane);
    const int i = lane & 15, kk = lane >> 4;
    const int e128 = L_KT + c * TBS + 2 * (0x4300 - 0x3F00);
    if (w & 1) {
      const int sh = i >> 3, base = e128 + (i < 8 ? 256 + (i >> 2) * 128 + (i & 3) * 2 : 256 + (i >> 1) * 64 + (i & 1) * 2) + (kk << (5 - sh));
#pragma unroll
      for (int e = 0; e < 4; ++e) A.sad[e] = base + (e << (3 - sh));
    } else {
      const float fb = (float)(128 * (i >> 3) + (i & 7) + 32 * kk);
      const u32 s0 = tb_addr(pk2(fb, fb + 8.f), sb), s1 = tb_addr(pk2(fb + 16.f, fb + 24.f), sb);
      A.sad[0] = s0 & 0xFFFFu; A.sad[1] = s0 >> 16; A.sad[2] = s1 & 0xFFFFu; A.sad[3] = s1 >> 16;
    }
    A.sad[4] = e128 + 768 + min(32 * i, 258) + 8 * kk;
    asm volatile("" : "+v"(A.sad[0]), "+v"(A.sad[1]), "+v"(A.sad[2]), "+v"(A.sad[3]), "+v"(A.sad[4]));
    __builtin_amdgcn_sched_barrier(0);
  };
  auto k_unit = [&](P0A& A, float dk, bool lag0) __attribute__((always_inline)) {
    f16x8 fr[8], fc[8];
    const f32x2 bd = *(const f32x2*)(smem + L_P0 + 512 + c * 8);
    __builtin_amdgcn_sched_barrier(0);
#pragma unroll
    for (int e = 0; e < 8; ++e) {
      fc[e] = *(const LAS f16x8*)(size_t)(A.ua + e * 64 + 41 * FTS);
      fr[e] = *(const LAS f16x8*)(size_t)(A.ur + e * 64);
      __builtin_amdgcn_sched_barrier(0);
    }
    f16x8 f2[8];
    p0_sad(A, lane);
    const int ec = bexp(bd[0] + bd[1] + fabsf(dk));
    float f0 = __uint_as_float((u32)max(127 + bexp(bd[0]) - ec, 0) << 23), f1 = __uint_as_float((u32)max(127 + bexp(bd[1]) - ec, 0) << 23);
    *(float*)(smem + L_MI + 4 * c) = __uint_as_float((u32)(ec - 14) << 23);
    float d0s = lag0 ? dk * __uint_as_float((u32)(268 - ec) << 23) : 0.f;
    asm volatile("" : "+v"(f0), "+v"(f1), "+v"(d0s));
    auto unit = [&](const f16x8 (&fr)[8], u32x4 sa, float add, bool more) __attribute__((always_inline)) {
      f32x4 k0, k1;
#pragma unroll
      for (int e = 0; e < 8; ++e) {
        if (e & 3) asm volatile("v_mfma_f32_16x16x32_f16 %0, %1, %2, %0" : "+v"(e < 4 ? k0 : k1) : "v"(fc[e]), "v"(fr[e]));
        else asm volatile("v_mfma_f32_16x16x32_f16 %0, %1, %2, 0" : "=&v"(e < 4 ? k0 : k1) : "v"(fc[e]), "v"(fr[e]));
        __builtin_amdgcn_sched_barrier(0);
        if (more) f2[e] = *(const LAS f16x8*)(size_t)(A.ua + e * 64 + 32 * FTS);
        __builtin_amdgcn_sched_barrier(0);
      }
      asm volatile("" : "+v"(k0));
      k0[0] = fmaf(k0[0], f0, add); k0[1] *= f0; k0[2] *= f0; k0[3] *= f0;
      asm volatile("s_nop 3" : "+v"(k0), "+v"(k1));
      const u32 h01 = pkh(fmaf(k1[0], f1, k0[0]), fmaf(k1[1], f1, k0[1])), h23 = pkh(fmaf(k1[2], f1, k0[2]), fmaf(k1[3], f1, k0[3]));
      *(LAS u16*)((LAS char*)(size_t)sa[0]) = (u16)h01; *(LAS u16*)((LAS char*)(size_t)sa[1]) = (u16)(h01 >> 16);
      *(LAS u16*)((LAS char*)(size_t)sa[2]) = (u16)h23; *(LAS u16*)((LAS char*)(size_t)sa[3]) = (u16)(h23 >> 16);
    };
    if (!(w & 1)) {
      unit(fr, (u32x4){A.sad[0], A.sad[1], A.sad[2], A.sad[3]}, d0s, true);
      cnt_add(smem, C_A, lane);
      unit(f2, (u32x4){A.sad[4], A.sad[4] + 2, A.sad[4] + 4, A.sad[4] + 6}, 0.f, false);
    } else {
      unit(fr, (u32x4){A.sad[0], A.sad[1], A.sad[2], A.sad[3]}, 0.f, false);
      cnt_add(smem, C_A, lane);
    }
  };
  auto k_copies = [&](const P0A& A, u32 cop, u32 keep) __attribute__((always_inline)) {
    u32 kh[11], kw[6], sh[5];
#pragma unroll
    for (int e = 0; e < 11; ++e) kh[e] = *(const LAS u16*)(size_t)A.gad[e];
    __builtin_amdgcn_sched_barrier(0);
#pragma unroll
    for (int e = 0; e < 5; ++e) kw[e] = __builtin_amdgcn_perm(kh[2 * e + 1], kh[2 * e], e == 4 ? keep : 0x05040100u);
    kw[5] = __builtin_amdgcn_perm(kh[10], kh[10], keep);
#pragma unroll
    for (int k = 0; k < 5; ++k) sh[k] = __builtin_amdgcn_alignbit(kw[k + 1], kw[k], 16);
    put_copies(cop, (u32x4){kw[0], kw[1], kw[2], kw[3]}, (u32x4){sh[0], sh[1], sh[2], sh[3]}, (u32x4){kw[1], kw[2], kw[3], kw[4]}, (u32x4){sh[1], sh[2], sh[3], sh[4]});
  };
  __builtin_amdgcn_sched_barrier(0);
  if (w < 4) {
#undef DROLE_
#define DROLE_ 0
  P0A pa;
  C1Pre c1;
  LAS char* wa = (LAS char*)(size_t)opaque((u32)(ftb + (tid & 127) * 4));
  u32 tg;
  const bool lo = bid < 128;
  const int xts = lo ? XBS : XTS, xt2 = lo ? 128 : 2 * XTS;
  const int xto = (int)opaque((u32)(lo ? OFF_X1 + ((bid >> 3) * 8 + 2 * w) * XBS + ((bid & 7) * 32 + 4 * (lane >> 3)) * 64 + (lane & 7) * 16
                                       : XTO((bid & 7) * 32 + 4 * (lane >> 3)) + (((bid >> 3) & 15) * 64 + 16 * w + 2 * (lane & 7)) * 8));
  int xta[4];
  auto xad = [&]() __attribute__((always_inline)) {
#pragma unroll
    for (int e = 0; e < 4; ++e) xta[e] = (int)opaque((u32)(xto + (e & 1) * xts + (e >> 1) * xt2));
  };
  xad();
  const int sl = (int)opaque((u32)(((lane & 7) * 8 + (lane >> 3)) * 4));
  auto xpub = [&]() __attribute__((always_inline)) {
    __builtin_amdgcn_sched_barrier(0);
    asm volatile("s_cmpk_gt_i32 %3, 0x7f\n\ts_waitcnt vmcnt(0)\n\ts_cbranch_scc1 0f\n\t"
                 "v_permlane32_swap_b32 v240, v241\n\tv_permlane32_swap_b32 v244, v245\n\tv_permlane32_swap_b32 v242, v243\n\tv_permlane32_swap_b32 v246, v247\n0:"
                 : "+{v[240:243]}"(fx[0]), "+{v[244:247]}"(fx[1]), "+{v[252:255]}"(fq) : "s"(bid) : "scc", "memory");
    u32x4 q[4];
#pragma unroll
    for (int e = 0; e < 8; ++e) q[e >> 1][2 * (e & 1)] = (u32)__builtin_amdgcn_ds_bpermute(sl, (int)fx[e & 1][e >> 1]);
    __builtin_amdgcn_sched_barrier(0);
    tg = fq[2] + 1;
    const u32 mk = x0_mark(tg);
    u32 m8[8];
#pragma unroll
    for (int e = 0; e < 8; ++e) { m8[e] = mk; asm volatile("" : "+v"(m8[e])); q[e >> 1][2 * (e & 1) + 1] = m8[e]; }
    __builtin_amdgcn_sched_barrier(0);
#pragma unroll
    for (int e = 0; e < 4; ++e) stw(wrs, xta[e], q[e]);
    asm volatile("s_nop 1" :: "v"(q[0]), "v"(q[1]), "v"(q[2]), "v"(q[3]) : "memory");
    ST(0, 3);
    __builtin_amdgcn_sched_barrier(0);
  };
  {
    float ar, ai;
    asm volatile("s_waitcnt vmcnt(3)\n\tv_lshlrev_b32 %0, 16, v248\n\tv_lshlrev_b32 %1, 16, v249" : "=&v"(ar), "=&v"(ai) : "{v[248:251]}"(fp) : "memory");
    asm volatile("s_waitcnt lgkmcnt(0)" : "+s"(ldtw), "+s"(dskw) :: "memory");
    ST(0, 1);
    const float dt = rbf(__expf(c ? hi2f(ldtw) : lo2f(ldtw)));
    const float zr = dt * ar * 1.4426950408889634f, zi = dt * ai * 0.15915494309189535f;
    float cs = __builtin_amdgcn_exp2f(fmaxf(zr, 0.f) * -1928.f);
    f32x2 P[8], Q1, R[17];
    { float a_, b_; cexp2(zr, zi, a_, b_); P[1] = (f32x2){a_, b_}; cexp2(zr * 128.f, zi * 128.f, a_, b_); Q1 = (f32x2){a_, b_}; }
    asm volatile("" : "+v"(cs), "+v"(P[1]), "+v"(Q1));
    P[2] = CMUL(P[1], P[1]);
    const f32x2 Q2 = CMUL(Q1, Q1);
    R[1] = Q1 * cs; R[2] = Q2 * cs;
    P[3] = CMUL(P[2], P[1]); P[4] = CMUL(P[2], P[2]);
    const f32x2 Q4 = CMUL(Q2, Q2);
    R[3] = CMUL(R[1], Q2); R[4] = Q4 * cs;
    P[5] = CMUL(P[4], P[1]); P[6] = CMUL(P[3], P[3]); P[7] = CMUL(P[4], P[3]);
#pragma unroll
    for (int j = 5; j <= 16; ++j) R[j] = CMUL(R[j - 4], Q4);
    asm volatile("" : "+v"(R[13]), "+v"(R[14]), "+v"(R[15]), "+v"(R[16]), "+v"(P[7]), "+v"(P[6]), "+v"(P[5]));
    xpub();
    PUT(0, cs, 0.f); PUT(8, R[1][0], R[1][1]);
#pragma unroll
    for (int r = 1; r < 8; ++r) { PUT(r, P[r][0] * cs, P[r][1] * cs); PUT(8 + r, CMR(R[1], P[r]), CMI(R[1], P[r])); }
    asm volatile("" ::: "memory");
#pragma unroll
    for (int j = 2; j < 4; ++j) {
      PUT(16 + 4 * (j - 2), R[j][0], R[j][1]);
#pragma unroll
      for (int r = 1; r < 4; ++r) PUT(16 + 4 * (j - 2) + r, CMR(R[j], P[2 * r]), CMI(R[j], P[2 * r]));
    }
#pragma unroll
    for (int j = 4; j < 8; ++j) { PUT(24 + 2 * (j - 4), R[j][0], R[j][1]); PUT(25 + 2 * (j - 4), CMR(R[j], P[4]), CMI(R[j], P[4])); }
#pragma unroll
    for (int j = 8; j <= 16; ++j) PUT(24 + j, R[j][0], R[j][1]);
    __builtin_amdgcn_sched_barrier(0);
  }
  ST(0, 2);
  cnt_add(smem, C_F, lane);
  PAGEV(0);
  pa.ua = (u32)(ftb + (lane & 15) * FTS + (lane >> 4) * 16); pa.ur = pa.ua + (w & 1) * 16 * FTS;
  asm volatile("" : "+v"(pa.ua), "+v"(pa.ur));
  p0_gad(pa, lane);
  cnt_spin(smem, C_F, 8);
  k_unit(pa, c ? hi2f(dskw) : lo2f(dskw), (tid & 127) == 0);
  u32x4 zz = {0, 0, 0, 0};
  asm volatile("" : "+v"(zz));
  c1_pre(c1, kp, w, lane);
  const u32 keep = opaque((tid & 127) == 127 ? 0x0c0c0c0cu : 0x05040100u);
  const u32 tl = opaque((u32)(((lane >> 1) & 7) * CSTR + 4096 + (lane & 1) * 16));
  c1.cop = opaque((u32)(c * 4 * CSTR + (tid & 127) * 16));
  cnt_spin(smem, C_A, 4);
  ST(0, 4);
  *(LAS u32x4*)(size_t)tl = zz;
  k_copies(pa, c1.cop + 2048, keep);
  asm volatile("" :: "v"(zz));
  cnt_add(smem, C_A, lane);
  ST(0, 5);
  cnt_spin(smem, C_A, 8);
  bf16x8 wof[16];
  u32x4 pfl, wfl;
  u32 g2, stag = __builtin_amdgcn_readfirstlane(tg);
  asm volatile("" : "+s"(stag));
  CPre cp;
  if (w & 1) a_conv1<1>(smem, wrs, kp, w, lane, c1, pfl, wfl, g2); else a_conv1<0>(smem, wrs, kp, w, lane, c1, pfl, wfl, g2);
  a_blayer<true>(smem, wrs, ws, out, tid, w, wof, stag, pfl, wfl, g2, cp);
  ST(1, 2);
  a_conv(smem, wrs, cp, w, lane);
  a_blayer<false>(smem, wrs, ws, out, tid, w, wof, stag, pfl, wfl, g2, cp);
  ST_OUT();
  asm volatile("s_endpgm\n\t.p2align 12" : "={v[240:243]}"(fx[0]), "={v[244:247]}"(fx[1]), "={v[248:251]}"(fp), "={v[252:255]}"(fq));
  }
  {
#undef DROLE_
#define DROLE_ 1
    asm volatile("s_load_dword %0, %1, %2" : "=s"(ldtw) : "s"(log_dt), "s"(4 * kp) : "memory");
    const int lane = lane_id();
    LAS char* wa = (LAS char*)(size_t)opaque((u32)(ftb + (w & 1) * 256 + lane * 4));
    u32 tag;
    u32 st0, st1;
    auto cols = [&]() __attribute__((always_inline)) {
    {
      float ar, ai, br, bi, cr, ci;
      asm volatile("v_lshlrev_b32 %0, 16, v248\n\tv_lshlrev_b32 %1, 16, v249\n\tv_lshlrev_b32 %2, 16, v250\n\t"
                   "v_lshlrev_b32 %3, 16, v251\n\tv_lshlrev_b32 %4, 16, v252\n\tv_lshlrev_b32 %5, 16, v253"
                   : "=&v"(ar), "=&v"(ai), "=&v"(br), "=&v"(bi), "=&v"(cr), "=&v"(ci) : "{v[248:251]}"(fp), "{v[252:255]}"(fq) : "memory");
      asm volatile("s_waitcnt lgkmcnt(0)" : "+s"(ldtw) :: "memory");
      const float dt = rbf(__expf(c ? hi2f(ldtw) : lo2f(ldtw)));
      const float dbr = dt * br, dbi = dt * bi;
      const float zr = dt * ar * 1.4426950408889634f, zi = dt * ai * 0.15915494309189535f;
      const float zp = fmaxf(zr, 0.f), g1 = __builtin_amdgcn_exp2f(zp * 1928.f);
      const float wr = (cr * dbr - ci * dbi) * g1, wi = (cr * dbi + ci * dbr) * g1;
      float bd = (fabsf(wr) + fabsf(wi)) * __builtin_amdgcn_exp2f(zp * 120.f);
      f32x2 S1, C[16];
      { float a_, b_; cexp2(zr * 8.f, zi * 8.f, a_, b_); S1 = (f32x2){a_, b_}; }
      bd = dpp_add<0x111, 0xf>(bd); bd = dpp_add<0x112, 0xf>(bd);
      const f32x2 S2 = CMUL(S1, S1);
      bd = dpp_add<0x114, 0xf>(bd); bd = dpp_add<0x118, 0xf>(bd);
      const f32x2 S4 = CMUL(S2, S2);
      bd = dpp_add<0x142, 0xa>(bd); bd = dpp_add<0x143, 0xc>(bd);
      bd = __int_as_float(__builtin_amdgcn_readlane(__float_as_int(bd), 63));
      *(float*)(smem + L_P0 + 512 + g * 4) = bd;
      const float sc = __uint_as_float((u32)(268 - bexp(bd)) << 23);
      C[0] = (f32x2){wr * sc, wi * sc};
      C[1] = CMUL(C[0], S1); C[2] = CMUL(C[0], S2); C[3] = CMUL(C[1], S2);
#pragma unroll
      for (int e = 4; e < 16; ++e) C[e] = CMUL(C[e - 4], S4);
#pragma unroll
      for (int e = 0; e < 16; ++e) *(LAS u32*)(wa + (41 + e) * FTS) = pkhn(C[e][0], C[e][1]);
    }
      cnt_add(smem, C_F, lane);
      ST(0, 2);
    };
    auto stats = [&]() __attribute__((always_inline)) {
    {
      const u32 ONES = opaque(0x3F803F80u);
      u32x4 (&xt)[2] = fx;
      float sm[2] = {0.f, 0.f}, sq[2] = {0.f, 0.f};
#pragma unroll
      for (int e = 0; e < 8; ++e) { sm[e & 1] = dot2acc(xt[e & 1][e >> 1], ONES, sm[e & 1]); sq[e & 1] = dot2acc(xt[e & 1][e >> 1], xt[e & 1][e >> 1], sq[e & 1]); }
      const bool b0 = lane & 1, b1 = lane & 2;
      const float v0 = (b0 ? sm[1] : sm[0]) + dpp_mov<DPP_XOR1>(b0 ? sm[0] : sm[1]), v1 = (b0 ? sq[1] : sq[0]) + dpp_mov<DPP_XOR1>(b0 ? sq[0] : sq[1]);
      float v = (b1 ? v1 : v0) + dpp_mov<DPP_XOR2>(b1 ? v0 : v1);
      v += dpp_mov<0x124>(v); v += dpp_mov<DPP_ROR8>(v);
      auto r = __builtin_amdgcn_permlane16_swap(__float_as_uint(v), __float_as_uint(v), false, false);
      v = __uint_as_float(r[0]) + __uint_as_float(r[1]);
      auto q = __builtin_amdgcn_permlane32_swap(__float_as_uint(v), __float_as_uint(v), false, false);
      v = (__uint_as_float(q[0]) + __uint_as_float(q[1])) * (1.f / 512.f);
      st0 = pack_stats(v, fmaxf(dpp_mov<DPP_XOR2>(v) - v * v, 0.f));
      st1 = dpp_u<DPP_XOR1>(st0);
    }
    };
    auto publish = [&]() __attribute__((always_inline)) {
      asm volatile("v_add_u32 %0, 1, v254" : "=v"(tag) : "{v[252:255]}"(fq) : "memory");
      stw(wrs, lane == 0 ? OFF_SG + bid * 64 + g * 16 : WSB, (u32x4){st0, tag, st1, tag});
      __builtin_amdgcn_sched_barrier(0);
    };
#define VMW(n_) asm volatile("s_waitcnt vmcnt(%0)" :: "n"(n_) : "memory")
    __builtin_amdgcn_s_setprio(1);
    VISIT(0);
    asm volatile("s_waitcnt vmcnt(8)" : "+{v[240:243]}"(fx[0]), "+{v[244:247]}"(fx[1]) :: "memory");
    ST(0, 1);
    stats();
    VMW(7);
    publish();
    const bool isq = bid < 128;
    const int us = min(11 + (bid >> 3) / 3, 15), um = (bid >> 3) % 3;
    asm volatile("s_mov_b32 m0, %2\n\ts_nop 0\n\tglobal_load_lds_dwordx4 %0, %1"
                 :: "v"((isq ? (us * 32 + 8 * g) * 3072 + um * 1024 : (((bid >> 3) & 15) * 32 + 8 * g) * 1024) + (bid & 7) * 128 + (lane >> 3) * (isq ? 3072 : 1024) + (((lane & 7) ^ (isq ? (lane >> 3) & 6 : ((lane >> 3) & 2) | ((g & 1) << 2))) << 4)),
                    "s"(isq ? wqkv : wo), "s"(L_WR + g * 1024) : "memory");
    ST(0, 10);
    const rsrc_t qrs = __builtin_amdgcn_make_buffer_rsrc((void*)wqkv, 0, 1572864, 0x00020000);
    const int wvo = (int)opaque((u32)((8 * g + (lane >> 3)) * 3072 + hb * 128 + (((lane & 7) ^ ((lane >> 3) & 6)) << 4)));
#define WSO(s_) ((s_) < 4 ? 40960 + (s_) * 12288 : (s_) == 4 ? L_WX - L_WS : (s_) < 7 ? 8192 + ((s_) - 5) * 12288 : 32768 + ((s_) - 7) * 12288)
    auto wraw = [&](auto S_) __attribute__((always_inline)) { constexpr int s = decltype(S_)::value; w_dma(qrs, wvo, s * 98304, L_WS + WSO(s) + g * 1024); };
    VMW(3);
    ST(0, 20);
    cols();
    __builtin_amdgcn_s_setprio(0);
    static_for<0, R1D>(wraw);
    PAGE(8);
    VISIT(1); VISIT(2);
    VMW(3 * R1D);
    cnt_add(smem, C_B, lane);
    static_for<R1D, R1E>(wraw);
    cnt_spin(smem, C_B, 4);
    {
      const u32 tq = (u32)(L_WR + (lane >> 2) * 128 + (((2 * g + ((lane & 3) >> 1)) ^ ((lane >> 2) & 6)) << 4) + (lane & 1) * 8);
      const u32 to = (u32)(L_WR + (8 * (lane >> 4) + ((lane >> 2) & 3)) * 128 + (((2 * g + ((lane & 3) >> 1)) ^ (((lane >> 2) & 2) | ((lane >> 2) & 4))) << 4) + (lane & 1) * 8);
      const bf16x4 q0 = __builtin_amdgcn_ds_read_tr16_b64_v4i16((LAS bf16x4*)(size_t)(isq ? tq : to)), q1 = __builtin_amdgcn_ds_read_tr16_b64_v4i16((LAS bf16x4*)(size_t)(isq ? tq + 2048 : to + 512));
      stw(wrs, (bid < 120 ? OFF_WQ + us * 98304 + ((bid & 7) * 4 + g) * 3072 + um * 1024 : isq ? WSB : OFF_WO + (((bid & 7) * 16 + ((bid >> 3) & 15)) * 4 + g) * 1024) + lane * 16,
          __builtin_bit_cast(u32x4, __builtin_shufflevector(q0, q1, 0, 1, 2, 3, 4, 5, 6, 7)));
    }
    static_for<R1E, 5>(wraw);
    const int fso = lane == 0 ? OFF_ST0 + 4096 + (bid >> 7) * 4096 + ((bid & 7) * 4 + g) * 128 + ((bid >> 3) & 15) * 4 : WSB;
    ST(0, 15);
    const u32 stag = __builtin_amdgcn_readfirstlane(tag);
    bf16x8 wf[48];
    GPre gp;
    GSum gs_;
    {
      const int ln = lane;
      const int wbo = (int)opaque((u32)((hb * 4 + g) * 3072 + ln * 16));
      const int fo = (int)opaque((u32)(OFF_ST0 + 4096 + (hb * 4 + g) * 128 + (ln & 3) * 16));
      const u32 wga = opaque((u32)(L_WS + (ln >> 2) * 128 + (((2 * g + ((ln & 3) >> 1)) ^ ((ln >> 2) & 6)) << 4) + (ln & 1) * 8)), wgb = opaque(wga + 53248u);
      auto wget = [&](auto S_) __attribute__((always_inline)) {
        constexpr int s = decltype(S_)::value, o = WSO(s) >= 53248 ? WSO(s) - 53248 : WSO(s);
        w_get<3 * s, o>(wf[3 * s], WSO(s) >= 53248 ? wgb : wga);
        w_get<3 * s + 1, o + 4096>(wf[3 * s + 1], WSO(s) >= 53248 ? wgb : wga);
        w_get<3 * s + 2, o + 8192>(wf[3 * s + 2], WSO(s) >= 53248 ? wgb : wga);
      };
      const int r0 = 256 * g + 2 * ln;
      const int sto = (int)opaque((u32)(OFF_SG + r0 * 8)), xto = (int)opaque((u32)(XTO(kp) + r0 * 8));
      const int xlo = (int)opaque((u32)(OFF_X1 + (32 * g + (ln >> 2)) * XBS + kp * 64 + (ln & 3) * 16));
      u32 mk = x0_mark(stag);
      asm volatile("" : "+s"(mk));
#define GXO(e_) ((e_) < 2 ? xlo + (e_) * 16 * XBS : xto + ((e_) - 2) * 1024)
      auto wld = [&](auto S_) __attribute__((always_inline)) {
        constexpr int s = decltype(S_)::value == 11 ? 15 : decltype(S_)::value - 1;
        w_ld<3 * s>(wf[3 * s], wbo, wrs); w_ld<3 * s + 1>(wf[3 * s + 1], wbo, wrs); w_ld<3 * s + 2>(wf[3 * s + 2], wbo, wrs);
      };
      u32x4 gs[4], gx[4];
#define GO(e_) (((e_) & 1) * 1024 + ((e_) >> 1) * 8192)
#define G_BAD(h_) __any((NE2(gs[2 * (h_)][1], gs[2 * (h_)][3], stag) | NE2(gs[2 * (h_) + 1][1], gs[2 * (h_) + 1][3], stag) | NE2(gx[2 * (h_)][1], gx[2 * (h_)][3], mk) | NE2(gx[2 * (h_) + 1][1], gx[2 * (h_) + 1][3], mk)) != 0u)
      LAS char* const uw = (LAS char*)(size_t)opaque((u32)(L_U + (256 + r0) * 2));
      LAS char* const xw = (LAS char*)(size_t)opaque((u32)(L_X + r0 * 4));
      auto take = [&](auto H_) __attribute__((always_inline)) {
        constexpr int H = decltype(H_)::value;
        if (G_BAD(H)) {
          for (unsigned spins = 0;; ++spins) {
            FAILC(H ? 4 : 0);
            __builtin_amdgcn_s_sleep(SLP_U);
            asm volatile("" ::: "memory");
#pragma unroll
            for (int e = 2 * H; e < 2 * H + 2; ++e) { gs[e] = ldw(wrs, sto + GO(e)); gx[e] = ldw(wrs, GXO(e)); }
            if (!G_BAD(H) || spins > SPIN_CAP) break;
          }
        }
#pragma unroll
        for (int e = 2 * H; e < 2 * H + 2; ++e) {
          const float rs0 = __builtin_amdgcn_rcpf(hi2f(gs[e][0])), rs1 = __builtin_amdgcn_rcpf(hi2f(gs[e][2])), n0 = -lo2f(gs[e][0]) * rs0, n1 = -lo2f(gs[e][2]) * rs1;
          const int r = GO(e) / 8;
          *(LAS u32*)(uw + r * 2) = pkh(fmaf(lo2f(gx[e][0]), rs0, n0), fmaf(lo2f(gx[e][2]), rs1, n1));
          *(LAS u32*)(uw + 4608 + r * 2) = pkh(fmaf(hi2f(gx[e][0]), rs0, n0), fmaf(hi2f(gx[e][2]), rs1, n1));
          *(LAS u32x2*)(xw + r * 4) = (u32x2){gx[e][0], gx[e][2]};
        }
        __builtin_amdgcn_sched_barrier(0);
      };
      f32x4 (&cs)[2] = gs_.cs;
      u32x4 (&on)[3] = gs_.on;
      u32& ix = gs_.ix;
      auto wsum = [&](auto I_) __attribute__((always_inline)) { g_sum<decltype(I_)::value>(gs_, wf); };
      if (SLP_G) __builtin_amdgcn_s_sleep(SLP_G);
      asm volatile("" ::: "memory");
#define GLD(d_, r_, o_, so_) asm volatile("buffer_load_dwordx4 %0, %1, %2, %3 offen sc1" : "=&{" r_ "}"(d_) : "v"(o_), "s"(wrs), "s"(so_) : "memory")
      GLD(gs[0], "v[24:27]", sto, 0); GLD(gx[0], "v[28:31]", xlo, 0); GLD(gs[1], "v[32:35]", sto, GO(1)); GLD(gx[1], "v[36:39]", xlo, 16 * XBS);
      GLD(gs[2], "v[40:43]", sto, GO(2)); GLD(gx[2], "v[44:47]", xto, 0); GLD(gs[3], "v[48:51]", sto, GO(3)); GLD(gx[3], "v[52:55]", xto, 1024);
      ST(0, 5);
      {
        const u32 bm = (((g & 1) ? 0xFE0u : 0x1FF0u) << (ln & 15)) >> ((ln >> 4) * 8);
        u32x4 bw;
#pragma unroll
        for (int e = 0; e < 4; ++e) bw[e] = (((bm >> (2 * e)) & 1u) | (((bm >> (2 * e + 1)) & 1u) << 16)) * 0x3C00u;
        asm volatile("" : "+v"(bw));
        *(LAS u32x4*)(size_t)(u32)(L_BD + (g & 1) * 1024 + ln * 16) = bw;
        asm volatile("s_nop 1" :: "v"(bw) : "memory");
      }
      VMW(8 + 3 * (5 - R1E));
      __builtin_amdgcn_raw_buffer_store_b32(stag, wrs, fso, 0, 16);
      auto r2a = [&]() __attribute__((always_inline)) {
        cnt_spin(smem, C_A, 4);
        static_for<5, 7>(wraw);
      };
      auto pre = [&]() __attribute__((always_inline)) {
        const u32x4 SENT = {0xFFFFFFFFu, 0xFFFFFFFFu, 0xFFFFFFFFu, 0xFFFFFFFFu};
        const int l0 = 256 * g + 4 * ln;
        stw(wrs, OFF_X2 + (kp * XS_ + l0) * 4, SENT); stw(wrs, OFF_X2 + (kp * XS_ + l0 + 1024) * 4, SENT);
        stw(wrs, l0 < 32 ? OFF_ST2 + bid * 128 + ln * 16 : WSB, (u32x4){0, 0, 0, 0});
        __builtin_amdgcn_sched_barrier(0);
      };
      VMW(9);
      cnt_add(smem, C_W, ln);
      cnt_spin(smem, C_W, 4);
      ST(0, 4);
      __builtin_amdgcn_s_setprio(1);
      static_for<0, 5>(wget);
      asm volatile("s_waitcnt vmcnt(%4)" : "+{v[24:27]}"(gs[0]), "+{v[28:31]}"(gx[0]), "+{v[32:35]}"(gs[1]), "+{v[36:39]}"(gx[1]) : "n"(5) : "memory");
      take(IC<0>{});
      cnt_add(smem, C_U, ln);
      __builtin_amdgcn_s_setprio(0);
      __builtin_amdgcn_raw_buffer_store_b32(stag, wrs, ln == 0 ? OFF_ST0 + kp * 16 + g * 4 : WSB, 0, 16);
      asm volatile("s_waitcnt vmcnt(%4)" : "+{v[40:43]}"(gs[2]), "+{v[44:47]}"(gx[2]), "+{v[48:51]}"(gs[3]), "+{v[52:55]}"(gx[3]) : "n"(2) : "memory");
      ST(0, 3);
      pre();
      r2a();
      take(IC<1>{});
      cnt_add(smem, C_P2, ln);
      PAGE(9);
      VISIT(3); VISIT(10); VISIT(4);
      cnt_wait<1>(smem, C_S, 4);
      u32x4 fl;
      static_for<7, 7 + FLS>(wraw);
      if (SLP_FL) __builtin_amdgcn_s_sleep(SLP_FL);
      asm volatile("buffer_load_dwordx4 %0, %1, %2, 0 offen sc1" : "=&{v[56:59]}"(fl) : "v"(fo), "s"(wrs) : "memory");
      static_for<7 + FLS, 11>(wraw);
      ST(0, 7);
      {
        const u64 om[3] = {0x0030003000030003ull, 0x0300030000300030ull, 0x0003000303000300ull};
#pragma unroll
        for (int e = 0; e < 3; ++e) { const u32 v = __builtin_amdgcn_inverse_ballot_w64(om[e]) ? 0x3F803F80u : 0u; on[e] = (u32x4){v, v, v, v}; }
        ix = (ln & 1) ? 0xEEEEu : 0x4444u;
        cs[0] = cs[1] = (f32x4){0.f, 0.f, 0.f, 0.f};
        asm volatile("" : "+v"(on[0]), "+v"(on[1]), "+v"(on[2]), "+v"(ix), "+v"(cs[0]), "+v"(cs[1]));
      }
      asm volatile("s_waitcnt vmcnt(%1)" : "+{v[56:59]}"(fl) : "n"(3 * (4 - FLS)) : "memory");
      cnt_add(smem, C_W, ln);
      cnt_add(smem, C_P3, ln);
      ST(0, 17);
      tags_wait(fl, wrs, fo, stag, 15);
      static_for<11, HA>(wld);
      cnt_spin(smem, C_W, 8);
      static_for<5, 7>(wget);
      cnt_add(smem, C_G2, ln);
      ST(0, 6);
      static_for<0, CSA>(wsum);
      VMW(3 * (HA - 11));
      cnt_add(smem, C_W, ln);
      asm volatile("s_waitcnt lgkmcnt(1)" ::: "memory");
      static_for<CSA, 10>(wsum);
      cnt_spin(smem, C_W, 12);
      static_for<7, 11>(wget);
      ST(0, 9);
      g_pre(gp, g, true);
      const u32 wxo = opaque((u32)(L_WX + g * 3072 + ln * 16));
      VMW(3 * (HA - 13)); static_for<22, 24>(wsum); static_for<16, 17>(wsum);
#pragma unroll
      for (int e = 0; e < 3; ++e) *(LAS bf16x8*)(size_t)(wxo + e * 1024) = wf[45 + e];
      asm volatile("s_waitcnt lgkmcnt(3)" ::: "memory");
      static_for<10, 16>(wsum);
      cnt_wait<1>(smem, C_S, 8);
      static_for<HA, 16>(wld);
      ST(0, 8);
    }
    ConvPre pa2, pb2;
    float isc;
    u32 rp, rq;
    u32x4 xa, xb;
    g_blayer<true>(smem, wrs, g, stag, gp, gs_, wf);
    {
      const int ln2 = lane_id();
      const int l2 = 256 * g + 4 * ln2;
      const int xo2 = (int)opaque((u32)(OFF_X3 + (16 * g + (ln2 >> 2)) * XBS + kp * 64 + (ln2 & 1) * 32 + (ln2 & 2) * 8));
      isc = *(const float*)(smem + L_MI + 4 * (ln2 & 1));
      rp = (u32)(L_REDA + (ln2 & 1) * 8192 + (g * 64 + (ln2 ^ ((ln2 >> 3) & 3))) * 16); rq = rp ^ 16u;
      asm volatile("" : "+v"(isc), "+v"(rp), "+v"(rq));
      cnt_wait(smem, C_X2, 4);
      if (SLP_C2) __builtin_amdgcn_s_sleep(SLP_C2);
      u32x4 gg[4];
      ST(1, 2);
      x2_poll(wrs, kp, l2, xa);
      conv_unp(pa2, xa);
      if (SLP_S) __builtin_amdgcn_s_sleep(SLP_S);
      st2_poll(wrs, l2, gg);
      conv2_in(smem, l2, pa2, gg);
      ST(1, 3);
      lds_barrier();
      ST(1, 7);
      conv_xch(pa2);
      if (SLP_C3) __builtin_amdgcn_s_sleep(SLP_C3);
      xb = x2_ld(wrs, kp, l2 + 1024);
      st2_ld(wrs, l2 + 1024, gg);
      lds_barrier();
      ST(1, 8);
      f32x4 cq[4];
      conv_rd<0>(cq, rp, rq);
      stw(wrs, xo2, conv_epi(cq, isc, pa2));
      asm volatile("" : "+v"(xb), "+v"(gg[0]), "+v"(gg[1]), "+v"(gg[2]), "+v"(gg[3]) :: "memory");
      ST(1, 4);
      if (__any((umax4(0u, xb) == 0xFFFFFFFFu) | (st2_and(gg) == 0u))) {
        FAILC(18);
        xb = x2_ld(wrs, kp, l2 + 1024);
        st2_ld(wrs, l2 + 1024, gg);
        if (__any((umax4(0u, xb) == 0xFFFFFFFFu) | (st2_and(gg) == 0u))) {
          x2_poll(wrs, kp, l2 + 1024, xb);
          st2_poll(wrs, l2 + 1024, gg);
        }
        asm volatile("" : "+v"(xb), "+v"(gg[0]), "+v"(gg[1]), "+v"(gg[2]), "+v"(gg[3]));
      }
      ST(1, 1);
      conv_unp(pb2, xb);
      conv2_in(smem, l2 + 1024, pb2, gg);
      ST(1, 5);
      lds_barrier();
      ST(1, 6);
      const int rb = (bid & 7) * 4 + (bid >> 6);
      if (SLP_XA2) __builtin_amdgcn_s_sleep(SLP_XA2);
      x_dma(wrs, OFF_X3, 2 * rb, L_TA, g, ln2);
      PAGEV(13);
      conv_xch(pb2);
      g_pre(gp, g, false);
      XPre xp;
      x_pre(xp, 0, g, ln2, false);
      const int xo3 = (int)opaque((u32)(xo2 + 64 * XBS));
      lds_barrier();
      ST(1, 9);
      conv_rd<1024>(cq, rp, rq);
      auto epb = [&]() __attribute__((always_inline)) {
        asm volatile("" : "+v"(isc));
        stw(wrs, xo3, conv_epi(cq, isc, pb2));
        __builtin_amdgcn_sched_barrier(0);
      };
      { epb(); x_take<0, 1, false>(smem, wrs, OFF_X3, 2 * rb, g, ln2, xp, 0xFFFFFFFFu); }
    }
    g_blayer<false>(smem, wrs, g, stag, gp, gs_, wf);
    if ((bid | g) == 0) __builtin_amdgcn_raw_buffer_store_b32(stag, wrs, OFF_EP, 0, 16);
    asm volatile("s_barrier\n\ts_barrier" ::: "memory");
    ST_OUT();
  }
}

__device__ __attribute__((used, noinline)) void k_text_end() { asm volatile(".Lcw_end:"); }

extern "C" void kernel_launch(void* const* d_in, const int* in_sizes, int n_in, void* d_out, int out_size,
                              void* d_ws, size_t ws_size, hipStream_t stream) {
  (void)hipFuncSetAttribute((const void*)k_mega, hipFuncAttributeMaxDynamicSharedMemorySize, MEGA_LDS);
  k_mega<<<256, 512, MEGA_LDS, stream>>>((const u16*)d_in[0], (const u16*)d_in[1], (const u16*)d_in[2], (const u16*)d_in[3],
                                         (const u16*)d_in[4], (const u16*)d_in[5], (const u16*)d_in[6], (const u16*)d_in[7],
                                         (const u16*)d_in[8], (const u16*)d_in[10], (const u16*)d_in[11], (char*)d_ws,
                                         (u32*)d_out);
}
#if defined(__HIP_DEVICE_COMPILE__)
#pragma clang attribute pop
#endif
```

```cpp
#if defined(__HIP_DEVICE_COMPILE__)
#pragma clang attribute push (__attribute__((target("no-packed-fp32-ops"))), apply_to = function)
#endif
#include <hip/hip_runtime.h>
#include <hip/hip_bf16.h>
#include <stdint.h>
#define TIDX ((int)__builtin_amdgcn_workitem_id_x())
#define BIDX ((int)__builtin_amdgcn_workgroup_id_x())
#define ST(set, k)
#define ST_INIT()
#define ST_OUT()
#define KATTR
#define FAILC(k)
#define DROLE_ 0

typedef unsigned short u16;
typedef unsigned int u32;
typedef unsigned long long u64;
using bf16x8 = __attribute__((ext_vector_type(8))) short;
using bf16x4 = __attribute__((ext_vector_type(4))) short;
using f16x8 = __attribute__((ext_vector_type(8))) _Float16;
using f16x4 = __attribute__((ext_vector_type(4))) _Float16;
using f32x2 = __attribute__((ext_vector_type(2))) float;
using f32x4 = __attribute__((ext_vector_type(4))) float;
using u32x2 = __attribute__((ext_vector_type(2))) unsigned;
using u32x4 = __attribute__((ext_vector_type(4))) unsigned;
#define LAS __attribute__((address_space(3)))
#define GAS __attribute__((address_space(1)))

#define D_ 512
#define NS_ 128
#define XS_ 2080

#define MB_ (1 << 20)
#define XTO(kp_) (OFF_O1 + (((kp_) >> 4) & 1) * MB_ + ((kp_) >> 5) * 131072 + ((kp_) & 15) * 8192)
#define XTS 8192
#define OFF_ST0 (5 * MB_)
#define OFF_SG (OFF_ST0 + 32768)
#define OFF_ST2 (OFF_ST0 + 65536)
#define OFF_EP (OFF_ST0 + 131072)
#define OFF_WQ (9 * MB_ - 262144)
#define OFF_WO (10 * MB_ + 393216)
#define OFF_X1 (11 * MB_)
#define OFF_O1 (14 * MB_)
#define OFF_X2 (16 * MB_)
#define OFF_X3 (19 * MB_)
#define OFF_O2 (22 * MB_)
#define OFF_KV1 (24 * MB_)
#define OFF_KV2 (28 * MB_)
#define KVL 0
#define KVO(rb_, ly_, h_, hd_) (OFF_KV1 + (KVL == 0 ? (((ly_) * 2 + (h_)) * 1024 + (rb_) * 32 + (hd_)) * 2048 : KVL == 1 ? ((rb_) * 128 + ((ly_) * 2 + (h_)) * 32 + (hd_)) * 2048 : (((rb_) * 32 + (hd_)) * 4 + (ly_) * 2 + (h_)) * 2048))
#define WSB (32 * MB_)
#define XBS 16768
#define SPIN_CAP (1u << 17)
#define SLP_U 2
#define SLP_F 2
#define SLP_G 1
#define R1D 3
#define R1E 4
#define FLS 1
#define HA 13
#define WOE 4
#define CSA 3
#define SLP_H1 0
#define SLP_H2 2
#define SLP_FL 2
#define OSA 17
#define OP2 5
#define KV1 51
#define KV2 51
#define KW1 76
#define KW2 76
#define KWN (LY ? KW2 : KW1)
#define PAGE(k_) do { asm volatile("s_branch 3f\n\t.p2align 12\n3:"); } while (0)
#define PAGEV(k_) do { asm volatile("s_branch 3f\n\t.p2align 12\n.Lpg%0:\n\ts_branch .Lbk%0\n3:" :: "n"(k_)); } while (0)
#define PAGEW(k_) do { asm volatile("s_branch 3f\n.Lpg%0:\n\ts_branch 4f\n\t.p2align 12\n4:\n\ts_branch .Lbk%0\n3:" :: "n"(k_)); } while (0)
#define VISIT(k_) asm volatile("s_branch .Lpg%0\n.Lbk%0:" :: "n"(k_))
#define GXH 3
#define GL1 2
#define SLP_XR1 0
#define SLP_XR2 0
#define SLP_XR3 0
#define SLP_XR4 0
#define SLP_OR 0
#define SIG 1
#define NSG 0
#define XEG 1
#define GPE 37
#define PA1 99
#define PA2 99
#define PB1 99
#define PB2 99
#define KP1 8
#define GR1 0
#define GE1 37
#define GT1 88
#define SG1 1
#define GPR 0
#define EX1 0
#define EX2 1
#define SLP_HG 1
#define NH1 89
#define NH2 93
#define SLP_HR 1
#define WO1 8
#define SLP_XA2 4
#define GPF 4
#define WSP 3
#define PFS2 22
#define PFD 3
#define SLP_XP 2
#define SLP_SP 4
#define SLP_S 2
#define SLP_XA1 0
#define SLP_XB1 5
#define SLP_XB2 8
#define SLP_OB1 4
#define SLP_OB2 9
#define SLP_OA1 8
#define SLP_OA2 8
#define SLP_C2 5
#define SLP_C3 10

__device__ __forceinline__ float bf2f(u16 v) { return __uint_as_float(((u32)v) << 16); }
typedef __attribute__((ext_vector_type(2))) __bf16 bf16x2v;
__device__ __forceinline__ u32 pk2(float a, float b) {
  f32x2 f = {a, b};
  bf16x2v v = __builtin_convertvector(f, bf16x2v);
  return __builtin_bit_cast(u32, v);
}
__device__ __forceinline__ float rbf(float f) { return __uint_as_float(pk2(f, 0.f) << 16); }
__device__ __forceinline__ float lo2f(u32 w) { return __uint_as_float(w << 16); }
__device__ __forceinline__ float hi2f(u32 w) { return __uint_as_float(w & 0xFFFF0000u); }
template <int CTRL>
__device__ __forceinline__ float dpp_mov(float v) {
  return __int_as_float(__builtin_amdgcn_update_dpp(0, __float_as_int(v), CTRL, 0xf, 0xf, false));
}
#define DPP_XOR1 0xB1
#define DPP_XOR2 0x4E
#define DPP_ROR8 0x128
template <int CTRL, int RMASK>
__device__ __forceinline__ float dpp_add(float v) {
  return v + __int_as_float(__builtin_amdgcn_update_dpp(0, __float_as_int(v), CTRL, RMASK, 0xf, false));
}
__device__ __forceinline__ float rmax(float a, float b) {
  float r;
  asm("v_max_f32 %0, %1, %2" : "=v"(r) : "v"(a), "v"(b));
  return r;
}
__device__ __forceinline__ void lds_barrier() {
  asm volatile("s_waitcnt lgkmcnt(0)" ::: "memory");
  __builtin_amdgcn_s_barrier();
  asm volatile("" ::: "memory");
}
__device__ __forceinline__ void cexp2(float re2, float rev, float& orr, float& oi) {
  float e = __builtin_amdgcn_exp2f(re2);
  rev = __builtin_amdgcn_fractf(rev);
  float s = __builtin_amdgcn_sinf(rev);
  float c = __builtin_amdgcn_cosf(rev);
  orr = e * c;
  oi = e * s;
}
__device__ __forceinline__ float dot2acc(u32 a, u32 b, float c) {
  return __builtin_amdgcn_fdot2_f32_bf16(__builtin_bit_cast(bf16x2v, a), __builtin_bit_cast(bf16x2v, b), c, false);
}
__device__ __forceinline__ u32 opaque(u32 v) { asm volatile("" : "+v"(v)); return v; }
__device__ __forceinline__ int lane_id() { return (int)__builtin_amdgcn_mbcnt_hi(~0u, __builtin_amdgcn_mbcnt_lo(~0u, opaque(0u))); }
__device__ __forceinline__ u32 pack_stats(float mean, float var) {
  float vb = rbf(var);
  float ve = rbf(vb + 1e-5f);
  float sd = __builtin_amdgcn_sqrtf(ve);
  return pk2(mean, sd);
}

#define SB() __builtin_amdgcn_sched_barrier(0)
template <int V> struct IC { static constexpr int value = V; };
template <int B, int E, class F>
__device__ __forceinline__ void static_for(F& f) { if constexpr (B < E) { f(IC<B>{}); static_for<B + 1, E>(f); } }
typedef __amdgpu_buffer_rsrc_t rsrc_t;
__device__ __forceinline__ u32x4 ldw(rsrc_t r, int off) { return __builtin_amdgcn_raw_buffer_load_b128(r, off, 0, 16); }
__device__ __forceinline__ u32x4 ldws(rsrc_t r, int off, int soff) { return __builtin_amdgcn_raw_buffer_load_b128(r, off, soff, 16); }
__device__ __forceinline__ void stw(rsrc_t r, int off, u32x4 v) { __builtin_amdgcn_raw_buffer_store_b128(v, r, off, 0, 16); }
__device__ __forceinline__ u32 umax4(u32 m, u32x4 v) { return max(max(max(m, v[0]), v[1]), max(v[2], v[3])); }
template <int CTRL>
__device__ __forceinline__ u32 dpp_u(u32 v) { return (u32)__builtin_amdgcn_update_dpp(0, (int)v, CTRL, 0xf, 0xf, false); }

#define CSTR 4160
#define L_U 33280
#define L_TA 42496
#define L_TB (L_TA + 32768)
#define L_QB (L_TB + 32768)
#define L_TH (L_QB + 16384)
#define L_SP (L_QB + 32768)
#define L_FS (L_SP + 4096)
#define L_MI (L_FS + 3072)
#define L_XS (L_MI + 80)
#define L_WX (L_XS + 1024)
#define L_BD (L_WX + 12288)
#define MEGA_LDS (L_BD + 2048)
#define L_O2 8192
#define L_REDA L_QB
#define L_REDB (L_QB + 16384)
#define L_XC L_REDB
#define L_X (L_REDB + 8192)
#define TBS 3136
#define L_WR (L_QB + 24576)
#define L_WS L_TA
#define L_KT L_TA
#define FTS 528
#define L_P0 (L_FS + 2048)
#define C_OB 20
#define C_HR 16
#define C_OT 24
#define C_X2 28
#define C_G2 32
#define C_A 36
#define C_B 40
#define C_F 44
#define C_KH 48
#define C_P2 52
#define C_XA 8
#define C_P3 64
#define C_U 56
#define C_S 72
#define C_W 60

typedef __attribute__((ext_vector_type(2))) _Float16 f16x2v;
typedef __attribute__((ext_vector_type(2))) short i16x2;
__device__ __forceinline__ u32 pkh(float a, float b) {
  u32 r;
  asm("v_cvt_pk_f16_f32 %0, %1, %2" : "=v"(r) : "v"(a), "v"(b));
  return r;
}
__device__ __forceinline__ u32 pkhn(float a, float b) {
  u32 r;
  asm("v_cvt_pk_f16_f32 %0, %1, -%2" : "=v"(r) : "v"(a), "v"(b));
  return r;
}
__device__ __forceinline__ u32 tb_addr(u32 bits2, u32 base2) {
  i16x2 b = __builtin_elementwise_max(__builtin_bit_cast(i16x2, bits2), (i16x2){0x3F00, 0x3F00});
  u32 r;
  asm("v_pk_mad_u16 %0, %1, 2, %2 op_sel_hi:[1,0,1]" : "=v"(r) : "v"(b), "s"(base2));
  return r;
}
__device__ __forceinline__ int bexp(float b) { return min(max((int)((__float_as_uint(b) >> 23) & 0xFF), 20), 240); }
__device__ __forceinline__ void cnt_add(char* smem, int off, int lane) {
  ST(2, 2);
  u32 t;
  asm volatile("s_mov_b64 exec, 1\n\tv_mov_b32 %0, %1\n\tds_inc_u32 %0, %0\n\ts_mov_b64 exec, -1" : "=v"(t) : "is"(L_MI + off) : "memory");
}
template <int SL = 2>
__device__ __forceinline__ void cnt_wait(char* smem, int off, u32 target) {
#pragma unroll 1
  for (unsigned n = 0; n < (1u << 23) && (u32)__builtin_amdgcn_readfirstlane(__hip_atomic_load((LAS u32*)(smem + L_MI + off), __ATOMIC_RELAXED, __HIP_MEMORY_SCOPE_WORKGROUP)) < target; ++n)
    __builtin_amdgcn_s_sleep(SL);
  asm volatile("" ::: "memory");
}
__device__ __forceinline__ void cnt_spin(char* smem, int off, u32 target) {
  u32 a, v, c, n;
  asm volatile("v_mov_b32 %0, %4\n\ts_brev_b32 %3, 64\n0:\n\tds_read_b32 %1, %0\n\ts_sub_u32 %3, %3, 1\n\ts_cbranch_scc1 1f\n\ts_waitcnt lgkmcnt(0)\n\tv_readfirstlane_b32 %2, %1\n\ts_cmp_lt_u32 %2, %5\n\ts_cbranch_scc1 0b\n1:"
               : "=&v"(a), "=&v"(v), "=&s"(c), "=&s"(n) : "is"(L_MI + off), "is"(target) : "scc", "memory");
  __builtin_amdgcn_s_waitcnt(0xC07F);
  ST(2, 3);
}
__device__ __forceinline__ u32 x0_mark(u32 tag) { return 0xFF807F80u + (((tag & 31u) + 1u) << 16) + ((tag >> 5) & 31u) + 1u; }
#define NE2(a_, b_, t_) __builtin_amdgcn_bitop3_b32(a_, b_, t_, 0x7e)
__device__ __forceinline__ bool tags_ok(const u32x4& f, u32 tag) { return !__any((NE2(f[0], f[1], tag) | NE2(f[2], f[3], tag)) != 0u); }
template <bool RARE = false>
__device__ __forceinline__ void tags_wait(u32x4& f, rsrc_t wrs, int fo, u32 tag, int fid) {
  if (RARE ? __builtin_expect(!tags_ok(f, tag), 0) : !tags_ok(f, tag)) {
    for (unsigned spins = 0; spins <= SPIN_CAP; ++spins) {
      FAILC(fid);
      __builtin_amdgcn_s_sleep(SLP_F);
      asm volatile("" ::: "memory");
      if (tags_ok(ldw(wrs, fo), tag)) break;
    }
  }
  asm volatile("" ::: "memory");
}
__device__ __forceinline__ void put_copies(u32 cop, u32x4 c0, u32x4 c1, u32x4 c2, u32x4 c3) {
  asm volatile("" : "+v"(c0), "+v"(c1), "+v"(c2), "+v"(c3));
  LAS u32x4* q = (LAS u32x4*)(size_t)cop;
  q[0] = c0; q[CSTR / 16] = c1; q[2 * CSTR / 16] = c2; q[3 * CSTR / 16] = c3;
  asm volatile("s_nop 1" :: "v"(c0), "v"(c1), "v"(c2), "v"(c3) : "memory");
}
struct ConvPre { f32x4 x0, x1; };
__device__ __forceinline__ void conv_ln(char* smem, int l0, ConvPre& P, f32x4 rs, f32x4 nm) {
  u32x2 u0 = {pkh(fmaf(P.x0[0], rs[0], nm[0]), fmaf(P.x0[1], rs[1], nm[1])), pkh(fmaf(P.x0[2], rs[2], nm[2]), fmaf(P.x0[3], rs[3], nm[3]))};
  u32x2 u1 = {pkh(fmaf(P.x1[0], rs[0], nm[0]), fmaf(P.x1[1], rs[1], nm[1])), pkh(fmaf(P.x1[2], rs[2], nm[2]), fmaf(P.x1[3], rs[3], nm[3]))};
  asm volatile("" : "+v"(u0), "+v"(u1));
  *(u32x2*)(smem + L_U + (256 + l0) * 2) = u0;
  *(u32x2*)(smem + L_U + 4608 + (256 + l0) * 2) = u1;
  asm volatile("s_nop 1" :: "v"(u0), "v"(u1) : "memory");
}
__device__ __forceinline__ void conv_unp(ConvPre& P, u32x4 xw) {
#pragma unroll
  for (int e = 0; e < 4; ++e) { P.x0[e] = lo2f(xw[e]); P.x1[e] = hi2f(xw[e]); }
  asm volatile("" : "+v"(P.x0), "+v"(P.x1));
}
__device__ __forceinline__ u32x4 x2_ld(rsrc_t wrs, int kp, int l0) { return ldw(wrs, OFF_X2 + (kp * XS_ + l0) * 4); }
__device__ __forceinline__ void x2_poll(rsrc_t wrs, int kp, int l0, u32x4& xw) {
#pragma unroll 1
  for (int spins = SPIN_CAP;; --spins) {
    asm volatile("" ::: "memory");
    xw = x2_ld(wrs, kp, l0);
    if (!__any(umax4(0u, xw) == 0xFFFFFFFFu && spins > 0)) break;
    FAILC(16 + (__builtin_amdgcn_readfirstlane(l0) >> 10));
    __builtin_amdgcn_s_sleep(SLP_XP);
  }
}
__device__ __forceinline__ void st2_ld(rsrc_t wrs, int l0, u32x4 (&g)[4]) {
#pragma unroll
  for (int e = 0; e < 4; ++e) g[e] = ldw(wrs, OFF_ST2 + ((l0 & ~255) + ((l0 >> 2) & 63)) * 16 + e * 1024);
}
__device__ __forceinline__ u32 st2_and(const u32x4 (&g)[4]) {
  u32 ok = 32u;
#pragma unroll
  for (int e = 0; e < 4; ++e) ok &= g[e][0] & g[e][2];
  return ok;
}
__device__ __forceinline__ void st2_poll(rsrc_t wrs, int l0, u32x4 (&g)[4]) {
#pragma unroll 1
  for (int spins = SPIN_CAP;; --spins) {
    asm volatile("" ::: "memory");
    st2_ld(wrs, l0, g);
    if (!__any(st2_and(g) == 0u && spins > 0)) break;
    FAILC(7 + (__builtin_amdgcn_readfirstlane(l0) >> 10));
    __builtin_amdgcn_s_sleep(SLP_SP);
  }
}
__device__ __forceinline__ void conv2_in(char* smem, int l0, ConvPre& P, u32x4 (&g)[4]) {
  f32x4 rs, nm;
#pragma unroll
  for (int e = 0; e < 4; ++e) {
    const float c1 = 1.0f / 512.0f, c2 = 1.0f / (512.0f * 4294967296.0f);
    const float m1 = fmaf((float)(int)g[e][1], c1, (float)g[e][0] * c2);
    const float qq = fmaf((float)(int)g[e][3], c1, fmaf((float)g[e][2], c2, 1e-5f));
    rs[e] = __builtin_amdgcn_rsqf(rmax(fmaf(-m1, m1, qq), 1e-5f));
    nm[e] = -m1 * rs[e];
  }
  conv_ln(smem, l0, P, rs, nm);
}
__device__ __forceinline__ void conv_xch(ConvPre& P) {
  const bool c = __builtin_amdgcn_inverse_ballot_w64(0xAAAAAAAAAAAAAAAAull);
  asm volatile("" : "+v"(P.x0), "+v"(P.x1));
#pragma unroll
  for (int e = 0; e < 4; ++e) {
    const float r = __int_as_float(__builtin_amdgcn_mov_dpp(__float_as_int(c ? P.x0[e] : P.x1[e]), DPP_XOR1, 0xf, 0xf, true));
    P.x0[e] = c ? r : P.x0[e]; P.x1[e] = c ? P.x1[e] : r;
  }
  asm volatile("" : "+v"(P.x0), "+v"(P.x1));
}
template <int O>
__device__ __forceinline__ void conv_rd(f32x4 (&q)[4], u32 r0, u32 r1) {
  const LAS f32x4 *p0 = (const LAS f32x4*)(size_t)r0 + O, *p1 = (const LAS f32x4*)(size_t)r1 + O;
  q[0] = p0[0]; q[1] = p0[256]; q[2] = p1[0]; q[3] = p1[256];
  __builtin_amdgcn_sched_barrier(0);
}
__device__ __forceinline__ u32x4 conv_epi(const f32x4 (&q)[4], float isc, const ConvPre& P) {
  const f32x4 a0 = q[0], a1 = q[1], b0 = q[2], b1 = q[3];
  u32x4 v;
#pragma unroll
  for (int e = 0; e < 2; ++e) {
    v[e] = pk2(fmaf(a1[2 * e], isc, fmaf(a0[2 * e], isc, P.x0[2 * e])), fmaf(a1[2 * e + 1], isc, fmaf(a0[2 * e + 1], isc, P.x0[2 * e + 1])));
    v[2 + e] = pk2(fmaf(b1[2 * e], isc, fmaf(b0[2 * e], isc, P.x1[2 * e])), fmaf(b1[2 * e + 1], isc, fmaf(b0[2 * e + 1], isc, P.x1[2 * e + 1])));
  }
  return v;
}
__device__ __forceinline__ void dma4k(rsrc_t r, int voff, int soff, int lds) {
  asm volatile("s_mov_b32 m0, %3\n\ts_nop 0\n\t"
               "buffer_load_dwordx4 %0, %1, %2 offen sc1 lds\n\t"
               "buffer_load_dwordx4 %0, %1, %2 offen offset:1024 sc1 lds\n\t"
               "buffer_load_dwordx4 %0, %1, %2 offen offset:2048 sc1 lds\n\t"
               "buffer_load_dwordx4 %0, %1, %2 offen offset:3072 sc1 lds"
               :: "v"(voff), "s"(r), "s"(soff), "s"(lds) : "memory");
}
__device__ __forceinline__ void x_dma(rsrc_t wrs, int xin, int rb16, int tile, int a, int lane) {
  const int vo = lane * 16;
  if (xin == OFF_X1) {
    if (tile == L_TB && !EX1) dma4k(wrs, ((lane >> 1) & 1) * (XBS - 32) + (lane >> 2) * 64 + 32 + (lane & 1) * 16, xin + (rb16 - 2) * XBS + a * 4096, L_TH + a * 4096);
#pragma unroll
    for (int e = 0; e < 4; ++e)
      asm volatile("s_mov_b32 m0, %3\n\ts_nop 0\n\tbuffer_load_dwordx4 %0, %1, %2 offen offset:%4 sc1 lds\n\tbuffer_load_dwordx4 %0, %1, %2 offen offset:%5 sc1 lds"
                   :: "v"(vo), "s"(wrs), "s"(xin + (rb16 + (e & 1)) * XBS + a * 4096), "s"(tile + (e & 1) * 16384 + a * 4096), "n"((e >> 1) * 2048), "n"((e >> 1) * 2048 + 1024) : "memory");
    return;
  }
  if (tile != L_TB || EX2) dma4k(wrs, vo, xin + (rb16 + 1) * XBS + a * 4096, tile + 16384 + a * 4096);
  dma4k(wrs, vo, xin + rb16 * XBS + a * 4096, tile + a * 4096);
  if (tile == L_TB && !EX2) {
    dma4k(wrs, vo, xin + (rb16 - 1) * XBS + a * 4096, L_TH + a * 4096);
    dma4k(wrs, vo, xin + (rb16 + 1) * XBS + a * 4096, tile + 16384 + a * 4096);
  }
}
__device__ __forceinline__ bf16x8 ld_tr8(const LAS char* p) {
  const bf16x4 lo = __builtin_amdgcn_ds_read_tr16_b64_v4i16((LAS bf16x4*)p), hi = __builtin_amdgcn_ds_read_tr16_b64_v4i16((LAS bf16x4*)(p + 512));
  return __builtin_shufflevector(lo, hi, 0, 1, 2, 3, 4, 5, 6, 7);
}

__device__ __forceinline__ void prefill_o2(rsrc_t wrs, int a, int lane, int e) {
  stw(wrs, OFF_O2 + a * 524288 + BIDX * 2048 + e * 1024 + lane * 16, (u32x4){0xFFFFFFFFu, 0xFFFFFFFFu, 0xFFFFFFFFu, 0xFFFFFFFFu});
}
struct Far { f16x8 u[2][2], bd[2], t[4], k4; f32x4 d[2][2]; u32x4 v[2]; };
#define FMA_(z_, d_, a_, b_) do { __builtin_amdgcn_sched_barrier(0); if (z_) asm volatile("v_mfma_f32_16x16x32_f16 %0, %1, %2, 0" : "=&v"(d_) : "v"(a_), "v"(b_)); \
      else asm volatile("v_mfma_f32_16x16x32_f16 %0, %1, %2, %0" : "+v"(d_) : "v"(a_), "v"(b_)); __builtin_amdgcn_sched_barrier(0); } while (0)
#define FKB(Q_, h_) ((h_) ? 3 - (Q_) : (Q_))
#define F_LU(F_, Q_, e_, ub_) F_.u[(e_) >> 1][(e_) & 1] = *(const LAS f16x8*)((ub_) + 512 * FKB(Q_, (e_) >> 1) - 16 + 16 * ((e_) & 1))
#define F_WS(F_, e_) FMA_(true, F_.d[(e_) >> 1][(e_) & 1], F_.u[(e_) >> 1][(e_) & 1], F_.bd[(e_) & 1])
#define F_CV(F_, e_) asm volatile("v_cvt_pk_f16_f32 %0, %1, %2" : "=v"(F_.v[(e_) >> 2][(e_) & 3]) : "v"(F_.d[(e_) >> 2][0][(e_) & 3]), "v"(F_.d[(e_) >> 2][1][(e_) & 3]))
#define F_MM(F_, Q_, e_, acc_) do { \
      if ((e_) < 4 - (Q_)) FMA_((e_) == 0, acc_[(3 - (e_)) & 3], F_.v[0], F_.t[(3 - (e_) - (Q_)) & 3]); \
      else if ((e_) < 5) FMA_(false, acc_[((e_) - 1) & 3], F_.v[1], F_.t[((e_) - 4 + (Q_)) & 3]); \
      else FMA_(false, acc_[FKB(Q_, (e_) == 6)], F_.k4, F_.u[(e_) == 6][1]); } while (0)
struct CPre { u32 ah, ah2, bp, ra; f16x8 fh[4][4]; };
#define LDA(d_, o_) do { f16x4 h0_ = *(const LAS f16x4*)((const LAS char*)(size_t)C.ah + 3776 - (o_)), h1_ = *(const LAS f16x4*)((const LAS char*)(size_t)C.ah2 + 3776 - (o_)); \
      d_ = __builtin_shufflevector(h0_, h1_, 0, 1, 2, 3, 4, 5, 6, 7); } while (0)
__device__ __forceinline__ void c_pre(CPre& C, int a, int lane) {
  lane = (int)opaque((u32)lane);
  const int c = a >> 1, qq = a & 1, j16 = lane & 15, kg = lane >> 4;
  const int y00 = 2031 - j16 + 8 * kg - 128 * qq;
  const int sg = y00 & 3;
  C.ah = opaque((u32)(c * 4 * CSTR + sg * CSTR + 2 * (y00 - sg) - 3776));
  C.ah2 = opaque(C.ah + 8);
  C.bp = opaque((u32)(L_U + c * 4608 + (256 + 16 * j16 - 16 + 8 * kg - 128 * qq) * 2 - 192));
  C.ra = opaque((u32)(L_REDA + (a * 256 + 4 * j16 + (kg ^ ((j16 >> 1) & 3) ^ c)) * 16));
#pragma unroll
  for (int e = 0; e < 16; ++e) { LDA(C.fh[e >> 2][e & 3], 512 * (e & 3) + 64 * (e >> 2)); __builtin_amdgcn_sched_barrier(0); }
#pragma unroll
  for (int e = 0; e < 16; ++e) asm volatile("" : "+v"(C.fh[e >> 2][e & 3]));
}
__device__ __forceinline__ void a_conv(char* smem, rsrc_t wrs, CPre& C, int a, int lane) {
  LAS f32x4* ra = (LAS f32x4*)(size_t)C.ra;
  f32x4 acc[8];
  f16x8 bf[4][8];
#define CSB() __builtin_amdgcn_sched_barrier(0)
#define LDB(r_, t_) bf[r_][t_] = *(const LAS f16x8*)((const LAS char*)(size_t)C.bp + 512 * (t_) + 192 - 64 * (r_))
#define CMF(z_, cb_, r_, mu_) do { CSB(); if (z_) asm volatile("v_mfma_f32_16x16x32_f16 %0, %1, %2, 0" : "=&v"(acc[cb_]) : "v"(C.fh[r_][mu_]), "v"(bf[r_][(cb_) - (mu_)])); \
      else asm volatile("v_mfma_f32_16x16x32_f16 %0, %1, %2, %0" : "+v"(acc[cb_]) : "v"(C.fh[r_][mu_]), "v"(bf[r_][(cb_) - (mu_)])); CSB(); } while (0)
  auto tri = [&](int T) __attribute__((always_inline)) {
#pragma unroll
    for (int e = 0; e < 8; ++e) { LDB(e < 4 ? 0 : (e - 4) % 3 + 1, T + (e < 4 ? e : (e - 4) / 3)); CSB(); }
    int i = 0;
#pragma unroll
    for (int mu = 0; mu < 4; ++mu)
#pragma unroll
      for (int cb = mu; cb < 4; ++cb, ++i) { CMF(!T && !mu, T + cb, 0, mu); if (i < 8) LDB((i + 4) % 3 + 1, T + (i + 4) / 3); }
#pragma unroll
    for (int cb = 0; cb < 4; ++cb)
#pragma unroll
      for (int e = 0; e < 3 * cb + 3; ++e) {
        CMF(false, T + cb, e % 3 + 1, cb - e / 3);
        if (cb && e == 1) ra[256 * T + (cb - 1) * 64] = acc[T + cb - 1];
      }
  };
  asm volatile("s_barrier" ::: "memory");
  ST(1, 7);
  tri(0);
  Far F;
  u32 tp, bq, ub[2];
#pragma unroll
  for (int n = 0; n < 28; ++n) {
    const int nd = n < 16 ? n : n - 4, rr = nd / 6, k = nd % 6, mu = k < 1 ? 1 : k < 3 ? 2 : 3, cb = 4 + k - mu * (mu - 1) / 2;
    if (n >= 16 && n < 20) F_WS(F, n & 3); else CMF(rr == 0 && cb - mu == 3, cb, rr, mu);
    if (n == 1) { ra[3 * 64] = acc[3]; asm volatile("" ::: "memory"); }
    if (n == 2) {
      asm volatile("s_waitcnt lgkmcnt(0)\n\ts_barrier" :: "v"(acc[3]) : "memory");
      ST(1, 8);
    }
    if (n == 3) { ub[0] = opaque(C.bp + (a & 1) * 768); ub[1] = opaque(C.bp - (a & 1) * 256); }
    if (n == 4) { tp = opaque((u32)((a >> 1) * 4 * CSTR + lane * 16)); bq = opaque((u32)(L_BD + lane * 16)); }
    if (n == 5) { C.ah = opaque(C.ah + (a & 1) * 256); C.ah2 = opaque(C.ah2 + (a & 1) * 256); }
    if (n >= 6 && n < 10) F_LU(F, 0, n - 6, (const LAS char*)(size_t)ub[(n - 6) >> 1] + 192);
    if (n >= 6 && n < 8) F.bd[n - 6] = *(const LAS f16x8*)(size_t)(bq + (n - 6) * 1024);
    static_assert(PFD > 0 && PFS2 + PFD < 28, "");
    if (n == PFS2 || n == PFS2 + PFD) prefill_o2(wrs, a, lane, n != PFS2);
    if (n >= 16 && n < 20) F.t[19 - n] = *(const LAS f16x8*)(size_t)(tp + (19 - n) * CSTR);
    if (n == 20) LDA(F.k4, 2048);
    if (n >= 21 && n < 25) { F_CV(F, 2 * (n - 21)); F_CV(F, 2 * (n - 21) + 1); }
  }
#define FM "v_mfma_f32_16x16x32_f16 "
  asm volatile("s_bitcmp1_b32 %13, 0\n\ts_cbranch_scc1 0f\n\t"
               FM "%3, %4, %9, 0\n\t" FM "%2, %4, %8, %2\n\t" FM "%1, %4, %7, %1\n\t" FM "%0, %4, %6, %0\n\t" FM "%3, %5, %6, %3\n\t" FM "%0, %10, %11, %0\n\t" FM "%3, %10, %12, %3\n\ts_branch 1f\n0:\n\t"
               FM "%3, %4, %8, 0\n\t" FM "%2, %4, %7, %2\n\t" FM "%1, %4, %6, %1\n\t" FM "%2, %5, %6, %2\n\t" FM "%3, %5, %7, %3\n\t" FM "%1, %10, %11, %1\n\t" FM "%2, %10, %12, %2\n1:"
               : "+v"(acc[4]), "+v"(acc[5]), "+v"(acc[6]), "=&v"(acc[7])
               : "v"(F.v[0]), "v"(F.v[1]), "v"(F.t[0]), "v"(F.t[1]), "v"(F.t[2]), "v"(F.t[3]), "v"(F.k4), "v"(F.u[0][1]), "v"(F.u[1][1]), "s"(a) : "scc");
  ST(1, 5);
  asm volatile("s_waitcnt vmcnt(0)\n\ts_barrier" ::: "memory");
  ST(1, 6);
  tri(4);
  asm volatile("s_nop 7" : "+v"(acc[7]));
  ra[1024 + 3 * 64] = acc[7];
  asm volatile("s_waitcnt lgkmcnt(0)\n\ts_barrier" :: "v"(acc[7]) : "memory");
  ST(1, 9);
}

struct C1Pre { u32 ah, ah2, bp, xp, xc, so, hv, cop; };
__device__ __forceinline__ void c1_pre(C1Pre& C, int kp, int a, int lane) {
  lane = (int)opaque((u32)lane);
  const int c = a >> 1, P = a & 1, j16 = lane & 15, kg = lane >> 4;
  const int y0 = 2031 - j16 + 8 * kg, sg = y0 & 3;
  C.ah = c * 4 * CSTR + sg * CSTR + 2 * (y0 - sg) - 4032; C.ah2 = C.ah + 8;
  C.bp = L_U + c * 4608 + (240 + 16 * j16 + 8 * kg) * 2 - 448;
  C.xp = L_X + j16 * 64 + kg * 16; C.xc = L_XC + c * 4096 + lane * 16;
  C.so = OFF_X1 + (16 * ((kg & 1) ? 3 - P : P) + (j16 & ~1) + c) * XBS + kp * 64 + ((j16 & 1) ^ c) * 32 + (kg >> 1) * 16;
  C.hv = L_KT + c * TBS + 2 * (0x4480 - 0x3F00 - 6 + 64 * P + 2 * j16 - 8 * kg);
  asm volatile("" : "+v"(C.ah), "+v"(C.ah2), "+v"(C.bp), "+v"(C.xp), "+v"(C.xc), "+v"(C.so), "+v"(C.hv));
}
template <int P>
__device__ __forceinline__ void a_conv1(char* smem, rsrc_t wrs, int kp, int a, int lane, const C1Pre& C, u32x4& fl, u32x4& wfl, u32& g2) {
  if (P) PAGEV(2); else PAGEW(1);
  const int c = a >> 1;
  const LAS char *ah = (const LAS char*)(size_t)C.ah, *ah2 = (const LAS char*)(size_t)C.ah2, *bp = (const LAS char*)(size_t)C.bp, *xp = (const LAS char*)(size_t)C.xp;
  LAS char* xc = (LAS char*)(size_t)C.xc;
  constexpr int CX = P, CY = 3 - P, NMU = 4 - P;
  int so = (int)C.so, fo0, fo1;
  const u32 mi = opaque(L_MI), mx = opaque(L_MI + 4 * c);
  u32 hw[2][5], hv = 0, h1 = 0;
#pragma unroll
  for (int e = 0; e < 10; ++e) hw[e / 5][e % 5] = *(const LAS u32*)(size_t)(C.hv + (e / 5) * 64 + (e % 5) * 4);
  if (P) { const LAS u16* q = (const LAS u16*)(size_t)(u32)(L_KT + c * TBS + 2 * (0x4480 - 0x3F00)); hv = q[0]; h1 = q[-1]; }
  const u32 sel = c ? 0x03020c0cu : 0x01000c0cu;
  const float isc = *(const float*)(smem + L_MI + 4 * c);
  f16x8 fa[8][5], fb[8][8];
  Far F;
  u32 bq;
  f32x4 X, Y, acc[4], pre[2], pq[2];
  u32x4 xr[2];
  u32 kh = 0;
#define LDA1(r_, mu_) do { const int ao_ = 4032 - 64 * (r_) - 512 * (mu_);                                             \
      f16x4 h0_ = *(const LAS f16x4*)(ah + ao_), h1_ = *(const LAS f16x4*)(ah2 + ao_);                                \
      fa[r_][mu_] = __builtin_shufflevector(h0_, h1_, 0, 1, 2, 3, 4, 5, 6, 7); __builtin_amdgcn_sched_barrier(0); } while (0)
#define LDB1(r_, t_) fb[r_][t_] = *(const LAS f16x8*)(bp + 512 * (t_) + 448 - 64 * (r_))
#define MF(z_, d_, r_, mu_, t_) do { if (z_) asm volatile("v_mfma_f32_16x16x32_f16 %0, %1, %2, 0" : "=&v"(d_) : "v"(fa[r_][mu_]), "v"(fb[r_][t_])); \
      else asm volatile("v_mfma_f32_16x16x32_f16 %0, %1, %2, %0" : "+v"(d_) : "v"(fa[r_][mu_]), "v"(fb[r_][t_])); } while (0)
#define SBR() __builtin_amdgcn_sched_barrier(0)
#define RO(i_) (((i_) + 4 * (1 - P)) & 7)
  auto tri = [&](auto H_) __attribute__((always_inline)) {
    constexpr int H = decltype(H_)::value, T = 4 * H;
    if (H == 0) {
#pragma unroll
      for (int i = 0; i < 2; ++i)
#pragma unroll
        for (int e = 0; e < NMU; ++e) { LDB1(RO(i), T + e); SBR(); }
    }
#pragma unroll
    for (int i = 0; i < 8; ++i) {
      const int r = RO(i), rn = RO((i + 2) & 7);
#pragma unroll
      for (int e = 0; e < 5; ++e) {
        const bool z = H == 0 && i == 0;
        if (e == 0 && !z) asm volatile("" : "+v"(fb[r][T + NMU - 1]));
        if (P == 0) { if (e == 0) MF(z, Y, r, 3, T); else if (e == 1) MF(z, X, r, 0, T); else MF(false, Y, r, 4 - e, T + e - 1); }
        else { if (e == 0) MF(z, Y, r, 2, T); else if (e == 1) MF(z, X, r, 1, T); else if (e == 2) MF(false, X, r, 0, T + 1); else if (e == 3) MF(false, Y, r, 1, T + 1); else MF(false, Y, r, 0, T + 2); }
        SBR();
        if (i < 6 && e < NMU) LDB1(rn, T + e);
        if (H == 0 && i >= 1 && i <= NMU && e == 4) LDA1(RO(7), i - 1);
#define CNT_LD(c_) kh = *(const volatile LAS u32*)(size_t)(mi + (c_))
#define CNT_CK(c_, f_) if (__builtin_expect(__builtin_amdgcn_readfirstlane(kh) < 4u, 0)) { FAILC(f_); cnt_spin(smem, c_, 4); }
        if (i == 2 * H && e == 4) { xr[0] = *(const LAS u32x4*)(xp + (T + CX) * 1024); xr[1] = *(const LAS u32x4*)(xp + (T + CY) * 1024); }
        if (H == 0) {
          if (i == 3 && e == 1) CNT_LD(C_KH);
          if (i == 5 && e == 4) CNT_CK(C_KH, 17);
          if (i == 7 && e == 0 && P) LDB1(RO(7), 3);
          if (i == 7 && e == 1 && P) LDA1(RO(7), 3);
        } else {
          if (i == 0 && e == 1) *(LAS f32x4*)(xc + P * 2048) = acc[1 - P];
          if (i == 0 && e == 4) { *(LAS f32x4*)(xc + P * 2048 + 1024) = acc[2 + P]; asm volatile("" :: "v"(acc[1 - P]) : "memory"); cnt_add(smem, C_XA + 4 * c, lane); }
          if (i == 1 && e == 1) asm volatile("" :: "v"(acc[2 + P]));
          if (i == 1 && e == 4) { fl = ldw(wrs, fo0); wfl = ldw(wrs, fo1); }
          if (i == 3 && e == 1) kh = *(const volatile LAS u32*)(size_t)(mx + C_XA);
          if (i == 4 && e == 4) {
            if (__builtin_expect(__builtin_amdgcn_readfirstlane(kh) < 2u, 0)) { FAILC(22); cnt_spin(smem, C_XA + 4 * c, 2); }
          }
          if (i == 4 && e == 4) { pq[0] = *(const LAS f32x4*)(xc + (1 - P) * 2048); pq[1] = *(const LAS f32x4*)(xc + (1 - P) * 2048 + 1024); }
          if (i == 6 && e == 2) CNT_LD(C_P3);
          if (i == 7 && e == 3) { CNT_CK(C_P3, 12); g2 = *(const volatile LAS u32*)(size_t)(mi + C_G2); }
        }
        if (i >= 6 && i < 7 + H && e < 4) {
#pragma unroll
          for (int t = 0; t < 2; ++t) {
            pre[t][e] = i == 7 ? fmaf(pq[t][e], isc, pre[t][e]) : __uint_as_float(__builtin_amdgcn_perm(xr[t][e], xr[t][e], sel));
            asm volatile("" : "+v"(pre[t][e]));
          }
        }
        SBR();
      }
    }
    u32 o[4];
    float t0, t1;
    asm volatile("v_fma_f32 %0, %6, %22, %14\n\tv_fma_f32 %4, %7, %22, %15\n\tv_fma_f32 %1, %8, %22, %16\n\tv_fma_f32 %5, %9, %22, %17\n\t"
                 "v_cvt_pk_bf16_f32 %0, %0, %4\n\tv_cvt_pk_bf16_f32 %1, %1, %5\n\ts_nop 1\n\t"
                 "v_fma_f32 %2, %10, %22, %18\n\tv_fma_f32 %4, %11, %22, %19\n\tv_fma_f32 %3, %12, %22, %20\n\tv_fma_f32 %5, %13, %22, %21\n\t"
                 "v_cvt_pk_bf16_f32 %2, %2, %4\n\tv_cvt_pk_bf16_f32 %3, %3, %5\n\ts_nop 0\n\t"
                 "v_permlane16_swap_b32 %0, %2\n\tv_permlane16_swap_b32 %1, %3"
                 : "=&v"(o[0]), "=&v"(o[1]), "=&v"(o[2]), "=&v"(o[3]), "=&v"(t0), "=&v"(t1)
                 : "v"(X[0]), "v"(X[1]), "v"(X[2]), "v"(X[3]), "v"(Y[0]), "v"(Y[1]), "v"(Y[2]), "v"(Y[3]),
                   "v"(pre[0][0]), "v"(pre[0][1]), "v"(pre[0][2]), "v"(pre[0][3]), "v"(pre[1][0]), "v"(pre[1][1]), "v"(pre[1][2]), "v"(pre[1][3]), "v"(isc));
    const u32x4 ov = {o[0], o[1], o[2], o[3]};
    stw(wrs, so, ov);
    if (H == 0) cnt_add(smem, C_S, lane);
    asm volatile("s_nop 1" :: "v"(ov) : "memory");
    SBR();
  };
#pragma unroll
  for (int i = 0; i < 7; ++i)
#pragma unroll
    for (int e = 0; e < NMU; ++e) { LDA1(RO(i), e); SBR(); }
  {
    const u64 tm[4] = {0xf000ff00fff0ffffull, 0xe000fe00ffe0fffeull, 0xc000fc00ffc0fffcull, 0x8000f800ff80fff8ull};
    u32x4 t[2];
#pragma unroll
    for (int e = 0; e < 8; ++e) {
      const u32 w_ = __builtin_amdgcn_perm(hw[e >> 2][3 - (e & 3)], hw[e >> 2][4 - (e & 3)], 0x07060100u);
      t[e >> 2][e & 3] = P == 0 && e < 4 && !__builtin_amdgcn_inverse_ballot_w64(tm[e & 3]) ? 0u : w_;
    }
    asm volatile("" : "+v"(t[0]), "+v"(t[1]));
    LAS u32x4* q = (LAS u32x4*)(size_t)(C.cop + P * (2 * CSTR - 1024));
    q[0] = t[0]; q[CSTR / 16] = t[1];
    asm volatile("s_nop 1" :: "v"(t[0]), "v"(t[1]) : "memory");
    if (P) {
      const bool z = __builtin_amdgcn_inverse_ballot_w64(0x8888888888888888ull);
      const u32 vv = z ? __builtin_amdgcn_perm(hv, hv, 0x01000100u) : 0u, vw = z ? __builtin_amdgcn_perm(h1, hv, 0x05040100u) : 0u, ut = vv & 0xFFFF0000u;
      put_copies((u32)(c * 4 * CSTR + (124 + (lane & 3)) * 16), (u32x4){0, ut, vv, vv}, (u32x4){0, vv, vv, vv}, (u32x4){ut, vv, vv, vv}, (u32x4){vv, vv, vv, vw});
    }
    cnt_add(smem, C_KH, lane);
  }
  ST(0, 6);
  cnt_spin(smem, C_U, 4);
  ST(0, 7);
  __builtin_amdgcn_s_setprio(1);
  tri(IC<0>{});
  ST(0, 8);
  auto ab = [&](auto N_) __attribute__((always_inline)) {
    constexpr int n = decltype(N_)::value, nd = n < 12 ? n : n >= 16 && n < 28 ? n - 4 : -1, rr = nd < 0 ? 0 : nd / 6, k = nd < 0 ? 0 : nd % 6;
    constexpr int mu = k < 1 ? 1 : k < 3 ? 2 : 3, cb = k - mu * (mu - 1) / 2, q = n < 25 ? 0 : n - 25;
    if (nd >= 0) MF(rr == 0 && cb + 4 - mu == 3, acc[cb], RO(7 - rr), mu, cb + 4 - mu);
    else if (n < 16) F_WS(F, n & 3);
    else F_MM(F, P, (n - 28) & 7, acc);
    SBR();
    if (n == 0) { bq = opaque((u32)(L_BD + lane * 16)); if (P) LDB1(RO(6), 3); }
    if (n == 1 && P) LDA1(RO(6), 3);
    if (n >= 2 && n < 4) { F_LU(F, P, n & 1, bp + 448); F.bd[n & 1] = *(const LAS f16x8*)(size_t)(bq + (n & 1) * 1024); }
    if (n >= 4 && n < 6) F_LU(F, P, (n - 2) & 3, bp + 448);
    if (n >= 7 && n < 10) LDB1(RO(5), (10 - n) & 3);
    if (n >= 13 && n < 16) LDB1(RO(4), (16 - n) & 3);
    if ((n == 10 || n == 16) && P) LDA1(RO(n == 10 ? 5 : 4), 3);
    if (n == 6) { so += 64 * XBS; asm volatile("" : "+v"(so)); }
    if (n == 11) fo0 = (int)opaque((u32)(OFF_ST0 + (a * 64 + lane) * 16));
    if (n == 12) fo1 = (int)opaque((u32)(OFF_ST0 + 8192 + ((kp & 7) * 4 + a) * 128 + (lane & 3) * 16));
    if (n == 17) CNT_LD(C_P2);
    if (n >= 18 && n < 22 - P) F.t[(21 - P - n) & 3] = *(const LAS f16x8*)(size_t)(C.cop + ((21 - P - n) & 3) * CSTR - P * 1024);
    if (n >= 17 && n < 25) F_CV(F, (n - 17) & 7);
    if (n == 22) { LDA1(0, 4); F.k4 = fa[0][4]; }
    if (n == 24) CNT_CK(C_P2, 23);
    if (n >= 25 && q < 2 * NMU) LDB1(RO(q / NMU), 4 + q % NMU);
    SBR();
  };
  static_for<0, 35>(ab);
  X = acc[CX]; Y = acc[CY];
  tri(IC<1>{});
  __builtin_amdgcn_s_setprio(0);
  ST(0, 9);
}

__device__ __forceinline__ float vsel_(float a, float b, u64 m) { float d; asm volatile("v_cndmask_b32_e64 %0, %1, %2, %3" : "=v"(d) : "v"(a), "v"(b), "s"(m)); return d; }
struct XPre { u32 p, q[2], spw, six; u32x4 one; u64 m[3]; };
__device__ __forceinline__ void x_pre(XPre& X, int H, int a, int lane, bool pb) {
  X.m[0] = 0xAAAAAAAAAAAAAAAAull; X.m[1] = 0xCCCCCCCCCCCCCCCCull; X.m[2] = 0xf0000f0000f0000full;
  X.p = (H ? L_TB : L_TA) + a * 4096 + lane * 8;
#pragma unroll
  for (int e = 0; e < 3; ++e) asm volatile("" : "+s"(X.m[e]));
  X.spw = L_SP + H * 1536 + (lane & 15) * 16 + a * 4 + (__builtin_amdgcn_inverse_ballot_w64(X.m[2]) ? 768 : 0);
  X.six = __builtin_amdgcn_inverse_ballot_w64(X.m[0]) ? 0xEEEEu : 0x4444u;
  X.one = (u32x4){0x3F803F80u, 0x3F803F80u, 0x3F803F80u, 0x3F803F80u};
  asm volatile("" : "+v"(X.p), "+v"(X.spw), "+v"(X.six), "+v"(X.one));
  if (pb) { X.q[0] = X.p + (lane & 4) * 4096; X.q[1] = X.q[0] ^ 32u; asm volatile("" : "+v"(X.q[0]), "+v"(X.q[1])); }
}
template <int HB, int NY, bool PB>
__device__ __forceinline__ void x_take(char* smem, rsrc_t wrs, int xin, int rb16, int a, int lane, const XPre& X, u32 sent) {
  constexpr int tile = HB ? L_TB : L_TA, H = HB && !(PB ? EX1 : EX2), NT = H ? 3 : 2;
  typedef __attribute__((ext_vector_type(16))) short s16;
#define XKO(t_) ((t_) == 2 ? L_TH - L_TB : (t_) * 16384)
#define XTL(o_) (H ? (PB ? ((o_) + 2) % 3 : (o_) == 0 ? 0 : 3 - (o_)) : 1 - (o_))
  float s = 0.f;
  f32x4 ss[3], sg[3];
  bf16x8 xf[4];
  auto rd = [&](int o) __attribute__((always_inline)) {
#pragma unroll
    for (int j = 0; j < 4; ++j)
      xf[j] = ld_tr8(PB && o >= H ? (const LAS char*)(size_t)X.q[XTL(H + (j >> 1))] + (2 * (o - H) + (j & 1)) * 1024 : (const LAS char*)(size_t)X.p + XKO(XTL(o)) + j * 1024);
    __builtin_amdgcn_sched_barrier(0);
    if (!PB || o <= H) { ss[o] = (f32x4){0.f, 0.f, 0.f, 0.f}; asm volatile("" : "+v"(ss[o])); }
    if (PB && o == H) { ss[o + 1] = (f32x4){0.f, 0.f, 0.f, 0.f}; asm volatile("" : "+v"(ss[o + 1])); }
  };
  auto ex = [&](int o) __attribute__((always_inline)) {
    float d0, d1;
    if (o == NT - 1 && !PB) asm volatile("s_nop 3");
    asm volatile("v_cndmask_b32_e64 %0, %3, %4, %9\n\tv_cndmask_b32_e64 %1, %5, %6, %9\n\tv_cndmask_b32_e64 %0, %0, %1, %10\n\tv_add_f32 %2, %2, %0\n\t"
                 "v_add_f32 %1, %7, %8\n\tv_cndmask_b32_e64 %1, %1, %0, %11"
                 : "=&v"(d0), "=&v"(d1), "+v"(s) : "v"(sg[o][0]), "v"(sg[o][1]), "v"(sg[o][2]), "v"(sg[o][3]), "v"(ss[o][0]), "v"(ss[o][1]), "s"(X.m[0]), "s"(X.m[1]), "s"(X.m[2]), "v"(ss[o]));
    *(LAS float*)(size_t)(X.spw + XTL(o) * 256) = d1;
    __builtin_amdgcn_sched_barrier(0);
  };
  auto mm = [&](int o) __attribute__((always_inline)) {
    const bool u = PB && o >= H;
#pragma unroll
    for (int j = 0; j < 4; ++j) {
      const int q = u ? H + (j >> 1) : o;
      if (u ? o > H || (j & 1) : j) asm volatile("v_mfma_f32_16x16x32_bf16 %0, %1, %1, %0" : "+v"(sg[q]) : "v"(xf[j]));
      else asm volatile("v_mfma_f32_16x16x32_bf16 %0, %1, %1, 0" : "=&v"(sg[q]) : "v"(xf[j]));
      if (j & 1) asm volatile("v_smfmac_f32_16x16x64_bf16 %0, %1, %2, %3" : "+v"(ss[q]) : "v"(X.one), "v"((s16)__builtin_shufflevector(xf[j - 1], xf[j], 0, 1, 2, 3, 4, 5, 6, 7, 8, 9, 10, 11, 12, 13, 14, 15)), "v"(X.six));
    }
    __builtin_amdgcn_sched_barrier(0);
  };
  unsigned n = 0;
  u64 chk = ~0ull, bad;
  asm volatile("" : "+s"(n));
#pragma unroll 1
  do {
    if (n++) {
      FAILC((xin == OFF_X1 ? 1 : 9) + HB);
      if (xin == OFF_X1) __builtin_amdgcn_s_sleep(HB ? SLP_XR2 : SLP_XR1); else __builtin_amdgcn_s_sleep(HB ? SLP_XR4 : SLP_XR3);
      x_dma(wrs, xin, rb16, tile, a, lane);
      if (NY) asm volatile("s_waitcnt vmcnt(0)" ::: "memory");
      if (n > 4) {
        asm volatile("s_waitcnt vmcnt(0)" ::: "memory");
        bool st = false;
#pragma unroll 1
        for (int e = 0; e < 4 * NT; ++e) { const u32x4 v = *(const u32x4*)(smem + tile + a * 4096 + lane * 16 + XKO(e >> 2) + (e & 3) * 1024); st |= v[1] == sent || v[3] == sent; }
        if (!__any(st) || n > SPIN_CAP) chk = 0;
      }
    }
    asm volatile("s_waitcnt vmcnt(%0)" :: "n"(4 * (NT - 1) + NY) : "memory");
    s = 0.f;
    rd(0); mm(0);
#define XBAD() (__builtin_amdgcn_ballot_w64(s != s) & chk)
    bad = 0;
    constexpr bool EG = NT == 3 || (HB && !PB && XEG);
    if (EG) { asm volatile("s_nop 3"); ex(0); bad = XBAD(); }
    if (!bad) {
      asm volatile("s_waitcnt vmcnt(%0)" :: "n"(4 * (NT - 2) + NY) : "memory");
      rd(1);
      if (!EG && !PB) ex(0);
      mm(1);
      if (NT == 3) { asm volatile("s_waitcnt vmcnt(%0)" :: "n"(NY) : "memory"); rd(2); if (!PB) ex(1); mm(2); }
      if (PB) ex(NT - 2);
      ex(NT - 1);
      bad = XBAD();
    }
  } while (__builtin_expect(bad != 0, 0));
}
#define PIN(x_) asm volatile("" : "+v"(x_))
struct HaloMsg { u32x4 k, v; };
__device__ __forceinline__ void halo_ld(HaloMsg& h, rsrc_t wrs, int ho) { h.k = ldw(wrs, ho); h.v = ldw(wrs, ho + 1024); }
__device__ __forceinline__ void attention(u32x4 (&r)[3], HaloMsg& hm, int ho, u32 tag, rsrc_t wrs, int oo, bool first, int lane, u32x4 on, int fid) {
  const u64 VM0 = 0x0fff00ff000f0000ull, VM1 = 0x3fff03ff003f0003ull, VM2 = 0xf000ff00fff0ffffull, VM3 = 0xc000fc00ffc0fffcull;
  u64 PAR = 0xAAAAAAAAAAAAAAAAull, VF0 = first ? 0ull : VM0, VF1 = first ? 0ull : VM1, chk = ~0ull, bad;
  const u32 sl = (lane & 1) ? 0x01000c0cu : 0x0c0c0100u, su = (lane & 1) ? 0x03020c0cu : 0x0c0c0302u;
  u32 ni = 0xff800000u, sc = 0x3eb8aa3bu;
  asm volatile("" : "+s"(PAR));
  asm volatile("" : "+s"(sc));
  asm volatile("" : "+v"(ni));
  f32x4 l0, sh;
#define ATMP "v224", "v225", "v226", "v227", "v232", "v233", "v234", "v235", "v236", "v237", "v238", "v239"
  asm volatile(
      "v_mfma_f32_16x16x16_bf16 v[224:227], v[208:209], v[216:217], 0\n\t"
      "v_mfma_f32_16x16x16_bf16 v[220:223], v[218:219], v[216:217], 0\n\t"
      "v_mfma_f32_16x16x16_bf16 v[228:231], v[218:219], v[214:215], 0\n\t"
      "v_cndmask_b32_e64 v224, v224, v225, %[par]\n\t"
      "v_cndmask_b32_e64 v225, v226, v227, %[par]\n\t"
      "v_cndmask_b32_e64 v224, %[ni], v224, %[m2]\n\t"
      "v_cndmask_b32_e64 v225, %[ni], v225, %[m3]\n\t"
      "v_cndmask_b32_e64 v220, v220, v221, %[par]\n\t"
      "v_cndmask_b32_e64 v221, v222, v223, %[par]\n\t"
      "v_cndmask_b32_e64 v220, %[ni], v220, %[m0]\n\t"
      "v_cndmask_b32_e64 v221, %[ni], v221, %[m1]\n\t"
      "v_max_f32 v222, v224, v225\n\t"
      "v_max3_f32 v223, v222, v220, v221\n\t"
      "v_max3_f32 v222, v222, v220, v221\n\t"
      "v_cndmask_b32_e64 v228, v228, v229, %[par]\n\t"
      "v_cndmask_b32_e64 v229, v230, v231, %[par]\n\t"
      "v_permlane16_swap_b32 v222, v223\n\t"
      "v_max_f32 v226, v222, v223\n\t"
      "v_max_f32 v227, v222, v223\n\t"
      "v_cndmask_b32_e64 v228, %[ni], v228, %[m2]\n\t"
      "v_cndmask_b32_e64 v229, %[ni], v229, %[m3]\n\t"
      "v_permlane32_swap_b32 v226, v227\n\t"
      "v_max_f32 v222, v226, v227\n\t"
      "v_mul_f32_e64 v222, -v222, %[sc]\n\t"
      "v_fma_f32 v224, v224, %[sc], v222\n\t"
      "v_fma_f32 v225, v225, %[sc], v222\n\t"
      "v_exp_f32 v224, v224\n\t"
      "v_exp_f32 v225, v225\n\t"
      "v_fma_f32 v220, v220, %[sc], v222\n\t"
      "v_fma_f32 v221, v221, %[sc], v222\n\t"
      "v_exp_f32 v220, v220\n\t"
      "v_exp_f32 v221, v221\n\t"
      "v_cvt_pk_bf16_f32 v226, v224, v225\n\t"
      "v_cvt_pk_bf16_f32 v227, v220, v221\n\t"
      "v_perm_b32 v232, v226, v226, %[sl]\n\t"
      "v_perm_b32 v233, v226, v226, %[su]\n\t"
      "v_perm_b32 v234, v227, v227, %[sl]\n\t"
      "v_perm_b32 v235, v227, v227, %[su]\n\t"
      "s_nop 1\n\t"
      "v_mfma_f32_16x16x32_bf16 v[224:227], %[on], v[232:235], 0\n\t"
      "v_mfma_f32_16x16x32_bf16 v[236:239], v[210:213], v[232:235], 0\n\t"
      "s_nop 3\n\t"
      "v_rcp_f32 v222, v224\n\t"
      "s_nop 2\n\t"
      "v_mul_f32 v236, v236, v222\n\t"
      "v_mul_f32 v237, v237, v222\n\t"
      "v_mul_f32 v238, v238, v222\n\t"
      "v_mul_f32 v239, v239, v222\n\t"
      "v_cvt_pk_bf16_f32 v218, v236, v237\n\t"
      "v_cvt_pk_bf16_f32 v219, v238, v239\n\t"
      "v_max_f32 v230, v228, v229\n\t"
      : "+{v[216:219]}"(r[1]), "=&{v[228:231]}"(l0)
      : "{v[208:211]}"(r[0]), "{v[212:215]}"(r[2]), [par] "s"(PAR), [m0] "s"(VM0), [m1] "s"(VM1), [m2] "s"(VM2), [m3] "s"(VM3), [ni] "v"(ni), [sc] "s"(sc), [sl] "v"(sl), [su] "v"(su), [on] "v"(on)
      : "v220", "v221", "v222", "v223", ATMP, "memory");
  unsigned n = 0;
  asm volatile("" : "+s"(n), "+s"(VF0));
  asm volatile("" : "+s"(VF1));
#pragma unroll 1
  do {
    if (n++) {
      FAILC(fid);
      __builtin_amdgcn_s_sleep(SLP_HR);
      asm volatile("" ::: "memory");
      halo_ld(hm, wrs, ho);
      if (n > SPIN_CAP) chk = 0;
    }
    asm volatile(
      "v_bitop3_b32 v232, v241, v243, %[tag] bitop3:0x7e\n\t"
      "v_mov_b32 v241, v242\n\t"
      "v_bitop3_b32 v233, v245, v247, %[tag] bitop3:0x7e\n\t"
      "v_mov_b32 v210, v244\n\t"
      "v_mfma_f32_16x16x16_bf16 v[220:223], v[240:241], v[214:215], 0\n\t"
      "v_mov_b32 v211, v246\n\t"
      "v_or_b32 v232, v232, v233\n\t"
      "v_cmp_ne_u32_e64 %[bad], 0, v232\n\t"
      : "+{v[240:243]}"(hm.k), "+{v[208:211]}"(r[0]), "=&{v[220:223]}"(sh), [bad] "=&s"(bad) : "{v[244:247]}"(hm.v), "{v[212:215]}"(r[2]), [tag] "s"(tag) : "v232", "v233");
    bad &= chk;
  } while (__builtin_expect(bad != 0, 0));
  asm volatile(
      "s_nop 2\n\t"
      "v_cndmask_b32_e64 v220, v220, v221, %[par]\n\t"
      "v_cndmask_b32_e64 v221, v222, v223, %[par]\n\t"
      "v_cndmask_b32_e64 v220, %[ni], v220, %[m0]\n\t"
      "v_cndmask_b32_e64 v221, %[ni], v221, %[m1]\n\t"
      "v_max3_f32 v223, v230, v220, v221\n\t"
      "v_max3_f32 v222, v230, v220, v221\n\t"
      "s_nop 1\n\t"
      "v_permlane16_swap_b32 v222, v223\n\t"
      "v_max_f32 v226, v222, v223\n\t"
      "v_max_f32 v227, v222, v223\n\t"
      "s_nop 1\n\t"
      "v_permlane32_swap_b32 v226, v227\n\t"
      "v_max_f32 v222, v226, v227\n\t"
      "v_mul_f32_e64 v222, -v222, %[sc]\n\t"
      "v_fma_f32 v220, v220, %[sc], v222\n\t"
      "v_fma_f32 v221, v221, %[sc], v222\n\t"
      "v_exp_f32 v220, v220\n\t"
      "v_exp_f32 v221, v221\n\t"
      "v_fma_f32 v228, v228, %[sc], v222\n\t"
      "v_fma_f32 v229, v229, %[sc], v222\n\t"
      "v_exp_f32 v228, v228\n\t"
      "v_exp_f32 v229, v229\n\t"
      "v_cvt_pk_bf16_f32 v226, v220, v221\n\t"
      "v_cvt_pk_bf16_f32 v227, v228, v229\n\t"
      "v_perm_b32 v232, v226, v226, %[sl]\n\t"
      "v_perm_b32 v233, v226, v226, %[su]\n\t"
      "v_perm_b32 v234, v227, v227, %[sl]\n\t"
      "v_perm_b32 v235, v227, v227, %[su]\n\t"
      "s_nop 1\n\t"
      "v_mfma_f32_16x16x32_bf16 v[224:227], %[on], v[232:235], 0\n\t"
      "v_mfma_f32_16x16x32_bf16 v[236:239], v[210:213], v[232:235], 0\n\t"
      "s_nop 3\n\t"
      "v_rcp_f32 v222, v224\n\t"
      "s_nop 2\n\t"
      "v_mul_f32 v236, v236, v222\n\t"
      "v_mul_f32 v237, v237, v222\n\t"
      "v_mul_f32 v238, v238, v222\n\t"
      "v_mul_f32 v239, v239, v222\n\t"
      "v_cvt_pk_bf16_f32 v216, v236, v237\n\t"
      "v_cvt_pk_bf16_f32 v217, v238, v239\n\t"
      "s_nop 0\n\t"
      "v_permlane16_swap_b32 v216, v218\n\t"
      "v_permlane16_swap_b32 v217, v219\n\t"
      : "+{v[216:219]}"(r[1]), "+{v[228:231]}"(l0), "+{v[220:223]}"(sh)
      : "{v[208:211]}"(r[0]), "{v[212:215]}"(r[2]), [par] "s"(PAR), [m0] "s"(VF0), [m1] "s"(VF1), [ni] "v"(ni), [sc] "s"(sc), [sl] "v"(sl), [su] "v"(su), [on] "v"(on)
      : ATMP);
  stw(wrs, oo, r[1]);
  asm volatile("s_nop 1" :: "v"(r[1]) : "memory");
}
struct GCst { u64 PAR, VM[4]; u32 sc; };
__device__ __forceinline__ void attention_g(u32x4 (&r)[3], HaloMsg& hm, int ho, int hso, u32 tag, rsrc_t wrs, int oo, u32 sl, u32 su, u32 ni, u32x4 on, const GCst& K, int fid, f32x4 aq, f32x4 av, f32x4 tq, f32x4 tv, f32x4 rv, float rn) {
  const u64 PAR = K.PAR, VM0 = K.VM[0], VM1 = K.VM[1], VM2 = K.VM[2], VM3 = K.VM[3], VF0 = VM0, VF1 = VM1;
  const u32 sc = K.sc;
  u64 chk = ~0ull, bad;
  asm volatile("" : "+s"(chk));
  f32x4 l0 = tq, sh;
#define ATMG "v24", "v25", "v26", "v27", "v32", "v33", "v34", "v35", "v36", "v37", "v38", "v39"
  asm volatile(
      "s_nop 0\n\t"
      "v_mfma_f32_16x16x16_bf16 v[24:27], v[8:9], v[16:17], 0\n\t"
      "v_mfma_f32_16x16x16_bf16 v[20:23], v[18:19], v[16:17], 0\n\t"
      "v_fmac_f32 v28, %[aq0], %[rn]\n\t"
      "v_fmac_f32 v29, %[aq1], %[rn]\n\t"
      "v_fmac_f32 v30, %[aq2], %[rn]\n\t"
      "v_fmac_f32 v31, %[aq3], %[rn]\n\t"
      "v_cvt_pk_bf16_f32 v14, v28, v29\n\t"
      "v_cvt_pk_bf16_f32 v15, v30, v31\n\t"
      "v_cndmask_b32_e64 v24, v24, v25, %[par]\n\t"
      "v_cndmask_b32_e64 v25, v26, v27, %[par]\n\t"
      "v_mfma_f32_16x16x16_bf16 v[28:31], v[18:19], v[14:15], 0\n\t"
      "v_cndmask_b32_e64 v24, %[ni], v24, %[m2]\n\t"
      "v_cndmask_b32_e64 v25, %[ni], v25, %[m3]\n\t"
      "v_cndmask_b32_e64 v20, v20, v21, %[par]\n\t"
      "v_cndmask_b32_e64 v21, v22, v23, %[par]\n\t"
      "v_cndmask_b32_e64 v20, %[ni], v20, %[m0]\n\t"
      "v_cndmask_b32_e64 v21, %[ni], v21, %[m1]\n\t"
      "v_max_f32 v22, v24, v25\n\t"
      "v_max3_f32 v23, v22, v20, v21\n\t"
      "v_max3_f32 v22, v22, v20, v21\n\t"
      "v_cndmask_b32_e64 v28, v28, v29, %[par]\n\t"
      "v_cndmask_b32_e64 v29, v30, v31, %[par]\n\t"
      "v_permlane16_swap_b32 v22, v23\n\t"
      "v_max_f32 v26, v22, v23\n\t"
      "v_max_f32 v27, v22, v23\n\t"
      "v_cndmask_b32_e64 v28, %[ni], v28, %[m2]\n\t"
      "v_cndmask_b32_e64 v29, %[ni], v29, %[m3]\n\t"
      "v_permlane32_swap_b32 v26, v27\n\t"
      "v_max_f32 v22, v26, v27\n\t"
      "v_mul_f32_e64 v22, -v22, %[sc]\n\t"
      "v_fma_f32 v24, v24, %[sc], v22\n\t"
      "v_fma_f32 v25, v25, %[sc], v22\n\t"
      "v_exp_f32 v24, v24\n\t"
      "v_exp_f32 v25, v25\n\t"
      "v_fma_f32 v20, v20, %[sc], v22\n\t"
      "v_fma_f32 v21, v21, %[sc], v22\n\t"
      "v_exp_f32 v20, v20\n\t"
      "v_exp_f32 v21, v21\n\t"
      "v_fmac_f32 v36, %[av0], %[rv0]\n\t"
      "v_fmac_f32 v37, %[av1], %[rv1]\n\t"
      "v_fmac_f32 v38, %[av2], %[rv2]\n\t"
      "v_fmac_f32 v39, %[av3], %[rv3]\n\t"
      "v_cvt_pk_bf16_f32 v26, v24, v25\n\t"
      "v_cvt_pk_bf16_f32 v27, v20, v21\n\t"
      "v_perm_b32 v32, v26, v26, %[sl]\n\t"
      "v_perm_b32 v33, v26, v26, %[su]\n\t"
      "v_perm_b32 v34, v27, v27, %[sl]\n\t"
      "v_perm_b32 v35, v27, v27, %[su]\n\t"
      "v_cvt_pk_bf16_f32 v12, v36, v37\n\t"
      "v_cvt_pk_bf16_f32 v13, v38, v39\n\t"
      "v_mfma_f32_16x16x32_bf16 v[24:27], %[on], v[32:35], 0\n\t"
      "s_nop 0\n\t"
      "v_mfma_f32_16x16x32_bf16 v[36:39], v[10:13], v[32:35], 0\n\t"
      "s_nop 3\n\t"
      "v_rcp_f32 v22, v24\n\t"
      "s_nop 2\n\t"
      "v_mul_f32 v36, v36, v22\n\t"
      "v_mul_f32 v37, v37, v22\n\t"
      "v_mul_f32 v38, v38, v22\n\t"
      "v_mul_f32 v39, v39, v22\n\t"
      "v_cvt_pk_bf16_f32 v18, v36, v37\n\t"
      "v_cvt_pk_bf16_f32 v19, v38, v39\n\t"
      "v_max_f32 v30, v28, v29\n\t"
      : "+{v[16:19]}"(r[1]), "+{v[28:31]}"(l0), "=&{v[12:15]}"(r[2]), "+{v[36:39]}"(tv)
      : "{v[8:11]}"(r[0]), [aq0] "v"(aq[0]), [aq1] "v"(aq[1]), [aq2] "v"(aq[2]), [aq3] "v"(aq[3]), [rn] "v"(rn),
        [av0] "v"(av[0]), [av1] "v"(av[1]), [av2] "v"(av[2]), [av3] "v"(av[3]),
        [rv0] "v"(rv[0]), [rv1] "v"(rv[1]), [rv2] "v"(rv[2]), [rv3] "v"(rv[3]), [par] "s"(PAR), [m0] "s"(VM0), [m1] "s"(VM1), [m2] "s"(VM2), [m3] "s"(VM3), [ni] "v"(ni), [sc] "s"(sc), [sl] "v"(sl), [su] "v"(su), [on] "v"(on)
      : "v20", "v21", "v22", "v23", "v24", "v25", "v26", "v27", "v32", "v33", "v34", "v35", "memory");
  unsigned n = 0;
  asm volatile("" : "+s"(n));
#pragma unroll 1
  do {
    if (n++) {
      FAILC(fid);
      __builtin_amdgcn_s_sleep(SLP_HG);
      asm volatile("" ::: "memory");
      hm.k = ldws(wrs, ho, hso); hm.v = ldws(wrs, ho, hso + 1024);
      asm volatile("" : "+v"(hm.k), "+v"(hm.v));
      if (n > SPIN_CAP) chk = 0;
    }
    asm volatile(
      "v_bitop3_b32 v32, v41, v43, %[tag] bitop3:0x7e\n\t"
      "v_mov_b32 v41, v42\n\t"
      "v_bitop3_b32 v33, v45, v47, %[tag] bitop3:0x7e\n\t"
      "v_mov_b32 v10, v44\n\t"
      "v_mfma_f32_16x16x16_bf16 v[20:23], v[40:41], v[14:15], 0\n\t"
      "v_mov_b32 v11, v46\n\t"
      "v_or_b32 v32, v32, v33\n\t"
      "v_cmp_ne_u32_e64 %[bad], 0, v32\n\t"
      : "+{v[40:43]}"(hm.k), "+{v[8:11]}"(r[0]), "=&{v[20:23]}"(sh), [bad] "=&s"(bad) : "{v[44:47]}"(hm.v), "{v[12:15]}"(r[2]), [tag] "s"(tag) : "v32", "v33");
    bad &= chk;
  } while (__builtin_expect(bad != 0, 0));
  if (fid & 1) ST(1, 15);
  if (SIG == 2) cnt_add(nullptr, C_HR, 0); else asm volatile("s_nop 2");
  asm volatile(
      "v_cndmask_b32_e64 v20, v20, v21, %[par]\n\t"
      "v_cndmask_b32_e64 v21, v22, v23, %[par]\n\t"
      "v_cndmask_b32_e64 v20, %[ni], v20, %[m0]\n\t"
      "v_cndmask_b32_e64 v21, %[ni], v21, %[m1]\n\t"
      "v_max3_f32 v23, v30, v20, v21\n\t"
      "v_max3_f32 v22, v30, v20, v21\n\t"
      "s_nop 1\n\t"
      "v_permlane16_swap_b32 v22, v23\n\t"
      "v_max_f32 v26, v22, v23\n\t"
      "v_max_f32 v27, v22, v23\n\t"
      "s_nop 1\n\t"
      "v_permlane32_swap_b32 v26, v27\n\t"
      "v_max_f32 v22, v26, v27\n\t"
      "v_mul_f32_e64 v22, -v22, %[sc]\n\t"
      "v_fma_f32 v20, v20, %[sc], v22\n\t"
      "v_fma_f32 v21, v21, %[sc], v22\n\t"
      "v_exp_f32 v20, v20\n\t"
      "v_exp_f32 v21, v21\n\t"
      "v_fma_f32 v28, v28, %[sc], v22\n\t"
      "v_fma_f32 v29, v29, %[sc], v22\n\t"
      "v_exp_f32 v28, v28\n\t"
      "v_exp_f32 v29, v29\n\t"
      "v_cvt_pk_bf16_f32 v26, v20, v21\n\t"
      "v_cvt_pk_bf16_f32 v27, v28, v29\n\t"
      "v_perm_b32 v32, v26, v26, %[sl]\n\t"
      "v_perm_b32 v33, v26, v26, %[su]\n\t"
      "v_perm_b32 v34, v27, v27, %[sl]\n\t"
      "v_perm_b32 v35, v27, v27, %[su]\n\t"
      "s_nop 1\n\t"
      "v_mfma_f32_16x16x32_bf16 v[24:27], %[on], v[32:35], 0\n\t"
      "v_mfma_f32_16x16x32_bf16 v[36:39], v[10:13], v[32:35], 0\n\t"
      "s_nop 3\n\t"
      "v_rcp_f32 v22, v24\n\t"
      "s_nop 2\n\t"
      "v_mul_f32 v36, v36, v22\n\t"
      "v_mul_f32 v37, v37, v22\n\t"
      "v_mul_f32 v38, v38, v22\n\t"
      "v_mul_f32 v39, v39, v22\n\t"
      "v_cvt_pk_bf16_f32 v16, v36, v37\n\t"
      "v_cvt_pk_bf16_f32 v17, v38, v39\n\t"
      "s_nop 0\n\t"
      "v_permlane16_swap_b32 v16, v18\n\t"
      "v_permlane16_swap_b32 v17, v19\n\t"
      : "+{v[16:19]}"(r[1]), "+{v[28:31]}"(l0), "+{v[20:23]}"(sh)
      : "{v[8:11]}"(r[0]), "{v[12:15]}"(r[2]), [par] "s"(PAR), [m0] "s"(VF0), [m1] "s"(VF1), [ni] "v"(ni), [sc] "s"(sc), [sl] "v"(sl), [su] "v"(su), [on] "v"(on)
      : ATMG);
  stw(wrs, oo, r[1]);
  asm volatile("s_nop 1" :: "v"(r[1]) : "memory");
}
__device__ __forceinline__ void o_dma(rsrc_t wrs, int og, int ot, int a, int lane) {
  dma4k(wrs, lane * 16, og + a * 8192, ot + a * 8192);
  dma4k(wrs, lane * 16, og + a * 8192 + 4096, ot + a * 8192 + 4096);
}
template <bool L1, bool LATE, int OT, class F>
__device__ __forceinline__ void wo_mma2(f32x4 (&acc)[2], char* smem, rsrc_t wrs, int og, int a, int lane, const bf16x8 (&wof)[16], u32 cnt, const f32x4 (&r0)[2], const u32 (&qa)[4], u32 sent, F side) {
  constexpr int ot = OT;
  u32x4 of[32];
#define LDO2(s_) of[s_] = *(const LAS u32x4*)((const LAS char*)(size_t)qa[(s_) >> 3] + (ot - (L1 ? L_TA : L_O2)) + ((s_) & 7) * 1024)
#define WMM(s_) do { SB(); if ((s_) < 2) asm volatile("v_mfma_f32_16x16x32_bf16 %0, %1, %2, %3" : "=&v"(acc[(s_) & 1]) \
        : "v"(L1 ? __builtin_bit_cast(bf16x8, of[s_]) : wof[0]), "v"(L1 ? wof[0] : __builtin_bit_cast(bf16x8, of[s_])), "v"(r0[(s_) & 1])); \
      else if (L1) asm volatile("v_mfma_f32_16x16x32_bf16 %0, %1, %2, %0" : "+v"(acc[(s_) & 1]) : "v"(of[s_]), "v"(wof[(s_) >> 1])); \
      else asm volatile("v_mfma_f32_16x16x32_bf16 %0, %1, %2, %0" : "+v"(acc[(s_) & 1]) : "v"(wof[(s_) >> 1]), "v"(of[s_])); SB(); } while (0)
  u32 cv;
  unsigned n = 0;
  u64 chk = ~0ull, bad;
  asm volatile("" : "+s"(n));
#pragma unroll 1
  do {
    if (n++) {
      FAILC((L1 ? 5 : 13) + (ot != L_TA));
      __builtin_amdgcn_s_sleep(SLP_OR);
      o_dma(wrs, og, ot, a, lane);
      if (n > 4) {
        asm volatile("s_waitcnt vmcnt(0)" ::: "memory");
        bool st = false;
#pragma unroll 1
        for (int s = 0; s < 8; ++s) { const u32x4 v = *(const u32x4*)(smem + ot + a * 8192 + lane * 16 + s * 1024); st |= v[1] == sent || v[3] == sent; }
        if (!__any(st) || n > SPIN_CAP) chk = 0;
      }
    }
    asm volatile("s_waitcnt vmcnt(0)" ::: "memory");
    if (!L1 && !LATE) ST(1, 4);
    u32 mx = 0;
#pragma unroll
    for (int s = 0; s < 8; ++s) { LDO2(s); SB(); }
    side(-1);
#pragma unroll
    for (int s = 0; s < 3; ++s) {
      WMM(s);
#pragma unroll
      for (int e = 3 * s; e < 3 * s + 3 && e < 8; ++e) mx = max(max(mx, of[e][1]), of[e][3]);
      asm volatile("" : "+v"(mx));
    }
    bad = __builtin_amdgcn_ballot_w64(mx >= 0xFF810000u) & chk;
  } while (__builtin_expect(bad != 0, 0));
  if (LATE) {
    cnt_add(smem, C_OT, lane);
#pragma unroll
    for (int s = 3; s < WSP; ++s) WMM(s);
    side(7);
    cnt_spin(smem, C_OT, cnt);
#pragma unroll
    for (int s = 8; s < WSP + 8; ++s) { LDO2(s); SB(); }
#pragma unroll
    for (int s = WSP; s < 32; ++s) { WMM(s); if (s + 8 < 32) LDO2(s + 8); }
    return;
  }
  if constexpr (!L1) {
    lds_barrier();
#pragma unroll
    for (int s = 3; s < 8; ++s) {
      if (s < 7) { LDO2(2 * s + 2); LDO2(2 * s + 3); }
      WMM(s);
    }
  } else {
  cnt_add(smem, C_OT, lane);
  WMM(3);
  WMM(4);
  u32 c_, k_;
#define WR2(a_, b_) "ds_read_b128 %" #a_ ", %[q] offset:%[o]+" #a_ "*1024\n\tds_read_b128 %" #b_ ", %[q] offset:%[o]+" #b_ "*1024\n\t"
  asm volatile("ds_read_b32 %[cv], %[ca]\n\t" WR2(0, 1)
               "v_mfma_f32_16x16x32_bf16 %[a1], %[f5], %[w2], %[a1]\n\t" WR2(2, 3)
               "v_mfma_f32_16x16x32_bf16 %[a0], %[f6], %[w3], %[a0]\n\t" WR2(4, 5)
               "v_mfma_f32_16x16x32_bf16 %[a1], %[f7], %[w3], %[a1]\n\t"
               "s_brev_b32 %[k], 64\n\ts_branch 1f\n"
               "0:\n\tds_read_b32 %[cv], %[ca]\n\t" WR2(0, 1) WR2(2, 3) WR2(4, 5)
               "s_sub_u32 %[k], %[k], 1\n\ts_cbranch_scc1 2f\n"
               "1:\n\ts_waitcnt lgkmcnt(6)\n\tv_readfirstlane_b32 %[c], %[cv]\n\ts_cmp_lt_u32 %[c], %[n]\n\ts_cbranch_scc1 0b\n2:"
               : "=&v"(of[8]), "=&v"(of[9]), "=&v"(of[10]), "=&v"(of[11]), "=&v"(of[12]), "=&v"(of[13]), [cv] "=&v"(cv), [c] "=&s"(c_), [k] "=&s"(k_), [a0] "+v"(acc[0]), [a1] "+v"(acc[1])
               : [q] "v"(qa[1]), [o] "n"(OT - L_TA), [ca] "v"(L_MI + C_OT), [n] "s"(cnt), [f5] "v"(of[5]), [f6] "v"(of[6]), [f7] "v"(of[7]), [w2] "v"(wof[2]), [w3] "v"(wof[3]) : "scc", "memory");
  SB();
  LDO2(14); LDO2(15);
  }
#pragma unroll
  for (int s = 8; s < 32; ++s) {
    if (L1 && s < 14) asm volatile("s_waitcnt lgkmcnt(7)");
    if (!L1 && s >= 28) WMM(s == 29 ? 30 : s == 30 ? 29 : s); else WMM(s);
    if (s + 8 < 32) LDO2(s + 8);
    side(s);
  }
  if (L1) asm volatile("s_nop %2" : "+v"(acc[0]), "+v"(acc[1]) : "n"(5));
}
struct WoPre { f32x4 r[2]; int so; u32 ao, xs, c[3]; u64 m[2]; };
template <bool L1>
__device__ __forceinline__ void wo_pre(WoPre& P, const u32x2 (&res)[2], char* ws, int H, int rb, int hb, int a, int lane) {
  const int i16 = lane & 15, kg = lane >> 4, R0 = 1024 * H + 32 * rb;
#pragma unroll
  for (int t = 0; t < 2; ++t) P.r[t] = (f32x4){lo2f(res[t][0]), hi2f(res[t][0]), lo2f(res[t][1]), hi2f(res[t][1])};
  if (L1) {
    P.so = OFF_X2 + ((32 * hb + 8 * a + (i16 >> 1)) * XS_ + R0 + 16 * (lane & 1) + 4 * kg) * 4;
    P.ao = (u32)(OFF_ST2 + ((R0 & ~255) + ((R0 >> 2) & 63) + 4 * (lane & 1) + kg + ((lane >> 2) & 3) * 64) * 16 + ((lane >> 1) & 1) * 8);
    P.xs = L_XS + H * 512 + lane * 8;
    asm volatile("" : "+v"(P.r[0]), "+v"(P.r[1]), "+v"(P.so), "+v"(P.ao), "+v"(P.xs));
    P.c[0] = 0x05040100u; P.c[1] = 0x07060302u; P.c[2] = ~63u; P.m[0] = 0xAAAAAAAAAAAAAAAAull; P.m[1] = 0x3333333333333333ull;
#pragma unroll
    for (int e = 0; e < 3; ++e) asm volatile("" : "+s"(P.c[e]));
#pragma unroll
    for (int e = 0; e < 2; ++e) asm volatile("" : "+s"(P.m[e]));
  } else {
    P.so = ((R0 + 8 * a + (lane >> 3)) * 256 + 32 * hb + 4 * (lane & 7)) * 4;
    P.ao = H * 4096 + i16 * 128 + (((2 * a + (kg >> 1)) ^ i16) & 7) * 16 + (kg & 1) * 8;
    P.xs = H * 4096 + a * 1024 + (lane >> 3) * 128 + ((lane ^ (lane >> 3)) & 7) * 16;
    asm volatile("" : "+v"(P.r[0]), "+v"(P.r[1]), "+v"(P.so), "+v"(P.ao), "+v"(P.xs));
  }
}
__device__ __forceinline__ void out_put(const f32x4 (&acc)[2], u32 ad) {
  u32x2 p0, p1;
  asm volatile("v_cvt_pk_bf16_f32 %0, %2, %3\n\tv_cvt_pk_bf16_f32 %1, %4, %5" : "=&v"(p0[0]), "=&v"(p0[1]) : "v"(acc[0][0]), "v"(acc[0][1]), "v"(acc[0][2]), "v"(acc[0][3]));
  asm volatile("ds_write_b64 %0, %1\n\ts_nop 4" :: "v"(ad), "v"(p0) : "memory");
  asm volatile("v_cvt_pk_bf16_f32 %0, %2, %3\n\tv_cvt_pk_bf16_f32 %1, %4, %5" : "=&v"(p1[0]), "=&v"(p1[1]) : "v"(acc[1][0]), "v"(acc[1][1]), "v"(acc[1][2]), "v"(acc[1][3]));
  asm volatile("ds_write_b64 %0, %1 offset:2048" :: "v"(ad), "v"(p1) : "memory");
}
__device__ __forceinline__ void out_st(u32x4 v, int so, u32* out) {
  __builtin_amdgcn_raw_buffer_store_b128(v, __builtin_amdgcn_make_buffer_rsrc((void*)out, 0, 2 * MB_, 0x00020000), so, 0, 16);
}
template <bool L1>
__device__ __forceinline__ void wo_epi(const f32x4 (&acc)[2], const WoPre& P, char* smem, rsrc_t wrs, char* ws, u32* out, u32 ONES) {
  if (L1) {
    u32 v[4], t[8], lo, hi;
    asm volatile(
        "v_cvt_pk_bf16_f32 %4, %8, %9\n\t"
        "v_cvt_pk_bf16_f32 %5, %10, %11\n\t"
        "v_cvt_pk_bf16_f32 %6, %12, %13\n\t"
        "v_cvt_pk_bf16_f32 %7, %14, %15\n\t"
        "s_mov_b64 vcc, %[m0]\n\t"
        "v_cndmask_b32_dpp %1, %4, %6, vcc quad_perm:[1,0,3,2] row_mask:0xf bank_mask:0xf\n\t"
        "v_cndmask_b32_dpp %3, %5, %7, vcc quad_perm:[1,0,3,2] row_mask:0xf bank_mask:0xf\n\t"
        "s_not_b64 vcc, vcc\n\t"
        "v_cndmask_b32_dpp %4, %6, %4, vcc quad_perm:[1,0,3,2] row_mask:0xf bank_mask:0xf\n\t"
        "v_cndmask_b32_dpp %5, %7, %5, vcc quad_perm:[1,0,3,2] row_mask:0xf bank_mask:0xf\n\t"
        "v_perm_b32 %0, %1, %4, %[sl]\n\t"
        "v_perm_b32 %1, %1, %4, %[sh]\n\t"
        "v_perm_b32 %2, %3, %5, %[sl]\n\t"
        "v_perm_b32 %3, %3, %5, %[sh]\n\t"
        : "=&v"(v[0]), "=&v"(v[1]), "=&v"(v[2]), "=&v"(v[3]), "=&v"(t[0]), "=&v"(t[1]), "=&v"(t[2]), "=&v"(t[3])
        : "v"(acc[0][0]), "v"(acc[0][1]), "v"(acc[0][2]), "v"(acc[0][3]), "v"(acc[1][0]), "v"(acc[1][1]), "v"(acc[1][2]), "v"(acc[1][3]),
          [m0] "s"(P.m[0]), [sl] "s"(P.c[0]), [sh] "s"(P.c[1]) : "vcc", "scc");
    stw(wrs, P.so, (u32x4){v[0], v[1], v[2], v[3]});
    asm volatile(
        "s_mov_b64 vcc, %[m1]\n\t"
        "v_cndmask_b32 %0, %[v0], %[on], vcc\n\t"
        "v_cndmask_b32 %1, %[v1], %[on], vcc\n\t"
        "v_cndmask_b32 %2, %[v2], %[on], vcc\n\t"
        "v_cndmask_b32 %3, %[v3], %[on], vcc\n\t"
        "v_dot2_f32_bf16 %0, %[v0], %0, 0\n\t"
        "v_dot2_f32_bf16 %1, %[v1], %1, 0\n\t"
        "v_dot2_f32_bf16 %2, %[v2], %2, 0\n\t"
        "v_dot2_f32_bf16 %3, %[v3], %3, 0\n\t"
        "v_cndmask_b32_dpp %4, %[v0], %[on], vcc quad_perm:[2,3,0,1] row_mask:0xf bank_mask:0xf\n\t"
        "v_cndmask_b32_dpp %5, %[v1], %[on], vcc quad_perm:[2,3,0,1] row_mask:0xf bank_mask:0xf\n\t"
        "v_cndmask_b32_dpp %6, %[v2], %[on], vcc quad_perm:[2,3,0,1] row_mask:0xf bank_mask:0xf\n\t"
        "v_cndmask_b32_dpp %7, %[v3], %[on], vcc quad_perm:[2,3,0,1] row_mask:0xf bank_mask:0xf\n\t"
        "v_dot2c_f32_bf16_dpp %0, %[v0], %4 quad_perm:[2,3,0,1] row_mask:0xf bank_mask:0xf\n\t"
        "v_dot2c_f32_bf16_dpp %2, %[v2], %6 quad_perm:[2,3,0,1] row_mask:0xf bank_mask:0xf\n\t"
        "v_dot2c_f32_bf16_dpp %1, %[v1], %5 quad_perm:[2,3,0,1] row_mask:0xf bank_mask:0xf\n\t"
        "v_dot2c_f32_bf16_dpp %3, %[v3], %7 quad_perm:[2,3,0,1] row_mask:0xf bank_mask:0xf\n\t"
        "s_nop 0\n\t"
        "v_add_f32_dpp %4, %0, %0 row_shl:4 row_mask:0xf bank_mask:0x5\n\t"
        "v_add_f32_dpp %5, %2, %2 row_shl:4 row_mask:0xf bank_mask:0x5\n\t"
        "v_add_f32_dpp %4, %1, %1 row_shr:4 row_mask:0xf bank_mask:0xa\n\t"
        "v_add_f32_dpp %5, %3, %3 row_shr:4 row_mask:0xf bank_mask:0xa\n\t"
        "s_nop 0\n\t"
        "v_add_f32_dpp %0, %4, %4 row_ror:8 row_mask:0xf bank_mask:0x3\n\t"
        "v_add_f32_dpp %0, %5, %5 row_ror:8 row_mask:0xf bank_mask:0xc\n\t"
        "v_fract_f32 %1, %0\n\t"
        "v_cvt_flr_i32_f32 %[hi], %0\n\t"
        "v_mul_f32 %1, 0x4f800000, %1\n\t"
        "v_cvt_u32_f32 %1, %1\n\t"
        "v_and_or_b32 %[lo], %1, %[cm], 1\n\t"
        : "=&v"(t[0]), "=&v"(t[1]), "=&v"(t[2]), "=&v"(t[3]), "=&v"(t[4]), "=&v"(t[5]), "=&v"(t[6]), "=&v"(t[7]), [lo] "=&v"(lo), [hi] "=&v"(hi)
        : [v0] "v"(v[0]), [v1] "v"(v[1]), [v2] "v"(v[2]), [v3] "v"(v[3]), [on] "v"(ONES), [m1] "s"(P.m[1]), [cm] "s"(P.c[2]) : "vcc", "memory");
    const u64 mine = ((u64)hi << 32) | lo;
    const u64 old = __hip_atomic_fetch_add((LAS u64*)(size_t)P.xs, mine, __ATOMIC_RELAXED, __HIP_MEMORY_SCOPE_WORKGROUP);
    asm volatile("v_and_b32 %0, 3, %1\n\tv_cmpx_eq_u32 3, %0\n\tglobal_atomic_add_x2 %2, %3, %4\n\ts_mov_b64 exec, 1\n\tds_inc_u32 %5, %5\n\ts_mov_b64 exec, -1"
                 : "=&v"(t[0]) : "v"((u32)old), "v"(P.ao), "v"(old + mine), "s"(ws), "v"(L_MI + C_X2) : "vcc", "memory");
  }
}
template <bool L1>
__device__ __forceinline__ void res_ld(u32x2 (&res)[2], const char* smem, int tile, int hb, int a, int lane) {
#pragma unroll
  for (int t = 0; t < 2; ++t) {
    const int o = tile + t * 16384 + (64 * hb + 16 * a) * 32;
    if (L1) res[t] = *(const u32x2*)(smem + o + (lane & 14) * 32 + ((lane & 1) - t) * 16384 + ((lane & 1) ^ t) * 32 + (lane >> 4) * 8);
    else res[t] = __builtin_bit_cast(u32x2, __builtin_amdgcn_ds_read_tr16_b64_v4i16((LAS bf16x4*)(size_t)(u32)(o + lane * 8)));
  }
}
template <bool L1>
__device__ __forceinline__ void a_blayer(char* smem, rsrc_t wrs, char* ws, u32* out, int t, int a, bf16x8 (&wof)[16], u32 tag, u32x4 fl, u32x4 wfl, u32 g2, CPre& C) {
  t = (int)opaque((u32)t);
  int lane = t & 63;
  const int XIN = L1 ? OFF_X1 : OFF_X3, OBUF = L1 ? OFF_O1 : OFF_O2;
  const int bid = BIDX;
  const int rb = (bid & 7) * 4 + (bid >> 6), hb = (bid >> 3) & 7;
  const u32 ONES = opaque(0x3F803F80u);
  XPre xp;
  const int og = OBUF + rb * 32768;
  u32x2 resa[2], resb[2];
  const int wfo = OFF_ST0 + 8192 + (hb * 4 + a) * 128 + (lane & 3) * 16;
#define LDWO(e_) wof[e_] = __builtin_bit_cast(bf16x8, ldws(wrs, ((hb * 64 + a) * 64 + lane) * 16, OFF_WO + ((4 * a + (e_)) & 15) * 4096))
  auto ld_wo = [&](int e0, int e1) __attribute__((always_inline)) {
#pragma unroll
    for (int e = e0; e < e1; ++e) LDWO(e);
    __builtin_amdgcn_sched_barrier(0);
  };
  if (L1) {
    const int fo = OFF_ST0 + t * 16;
    if (SLP_XA1) __builtin_amdgcn_s_sleep(SLP_XA1);
    asm volatile("" : "+v"(fl), "+v"(wfl));
    tags_wait<true>(fl, wrs, fo, tag, 19);
    if ((u32)__builtin_amdgcn_readfirstlane(g2) < 4u) cnt_spin(smem, C_G2, 4);
    x_dma(wrs, XIN, 2 * rb, L_TA, a, lane);
    cnt_add(smem, C_S, lane);
    PAGEV(3);
    tags_wait(wfl, wrs, wfo, tag, 6);
  }
  if (L1) asm volatile("" : "+v"(lane));
  const int oo = (int)opaque((u32)(OBUF + ((rb * 32 + (hb * 2 + (a >> 1)) * 2 + ((lane >> 4) & 1)) * 64 + (2 * (a & 1) + (lane >> 5)) * 16 + (lane & 15)) * 16));
  const u32 sent = L1 ? x0_mark(tag) : 0xFFFFFFFFu;
  if (L1 && WOE) {
    ld_wo(0, WOE);
  }
  if (L1) { x_pre(xp, 0, a, lane, true); x_take<0, WOE, true>(smem, wrs, XIN, 2 * rb, a, lane, xp, sent); }
  if (!L1) { PAGE(6); VISIT(11); }
  ST(!L1, 10);
  lds_barrier();
  ST(!L1, 11);
  res_ld<L1>(resa, smem, L_TA, hb, a, lane);
  auto pf_x3 = [&]() __attribute__((always_inline)) {
    const u32x4 SENT = {0xFFFFFFFFu, 0xFFFFFFFFu, 0xFFFFFFFFu, 0xFFFFFFFFu};
    const int xo = OFF_X3 + (16 * a + (lane >> 2)) * XBS + ((bid & 7) * 32 + (bid >> 3)) * 64 + (lane & 3) * 16;
    stw(wrs, xo, SENT); stw(wrs, xo + 64 * XBS, SENT);
  };
  if (L1) pf_x3();
  if ((L1 ? SLP_XB1 : SLP_XB2) > 0) __builtin_amdgcn_s_sleep(L1 ? SLP_XB1 : SLP_XB2);
  ST(!L1, 12);
  x_dma(wrs, XIN, 64 + 2 * rb, L_TB, a, lane);
  const int ho = (int)opaque((u32)(KVO((rb - 1) & 31, !L1, 0, hb * 4 + a) + lane * 16));
  const u32 qo = opaque((u32)(L_QB + a * 3072 + lane * 16));
  x_pre(xp, 1, a, lane, L1);
  x_take<1, 0, L1>(smem, wrs, XIN, 64 + 2 * rb, a, lane, xp, sent);
  ST(!L1, 13);
  HaloMsg hm;
  if (L1) ld_wo(WOE, WO1);
  lds_barrier();
  ST(!L1, 14);
  if ((L1 ? SLP_H1 : SLP_H2) > 0) __builtin_amdgcn_s_sleep(L1 ? SLP_H1 : SLP_H2);
  halo_ld(hm, wrs, ho);
  {
    u32x4 r[3];
#pragma unroll
    for (int e = 0; e < 3; ++e) r[e] = *(const LAS u32x4*)(size_t)(qo + 1024 * e);
    attention(r, hm, ho, tag, wrs, oo, rb == 0, lane, xp.one, L1 ? 3 : 11);
  }
  ST(!L1, 16);
  if (L1) PAGEV(4);
  if (L1) ld_wo(WO1, 16);
  res_ld<L1>(resb, smem, L_TB, hb, a, lane);
  if (L1) { VISIT(5); VISIT(13); } else PAGE(7);
  __builtin_amdgcn_s_sleep(L1 ? SLP_OA1 : SLP_OA2);
  if (!L1 || (EX1 && SG1)) cnt_wait<1>(smem, C_HR, !L1 && EX1 && SG1 ? 8 : 4);
  ST(!L1, 17);
  o_dma(wrs, og, L_TA, a, lane);
  WoPre wp;
  u32 qa[4];
#pragma unroll
  for (int e = 0; e < 4; ++e) qa[e] = opaque((u32)((L1 ? L_TA : L_O2) + ((a + e) & 3) * 8192 + lane * 16));
  wo_pre<L1>(wp, resa, ws, 0, rb, hb, a, lane);
  WoPre wq;
  if (!L1) wo_pre<L1>(wq, resb, ws, 1, rb, hb, a, lane);
  f32x4 acc[2];
  if (L1) __builtin_amdgcn_s_setprio(1);
  auto obq = [&]() __attribute__((always_inline)) {
    ST(!L1, 18);
    if (L1) wo_epi<L1>(acc, wp, smem, wrs, ws, out, ONES);
    if (L1) __builtin_amdgcn_s_setprio(0);
    if (L1) cnt_spin(smem, C_X2, 4);
    cnt_spin(smem, C_OB, L1 ? 4 : 8);
    o_dma(wrs, og + MB_, L1 ? L_TB : L_O2, a, lane);
  };
  wo_mma2<L1, !L1, L_TA>(acc, smem, wrs, og, a, lane, wof, L1 ? 4 : 12, wp.r, qa, sent, [&](int s) __attribute__((always_inline)) { if (s == 7) obq(); });
  if (L1) obq();
  if (L1) asm volatile("" : "+v"(lane));
  f32x4 aca[2] = {acc[0], acc[1]};
  const int soa = wp.so;
  const u32 xsa = wp.xs, aoa = wp.ao;
  u32x4 oa;
  if (L1) wo_pre<L1>(wp, resb, ws, 1, rb, hb, a, lane); else wp = wq;
  if (L1) c_pre(C, a, lane);
  if (L1) PAGEV(5);
  ST(!L1, 19);
  if (!L1) asm volatile("" : "+v"(aca[0]), "+v"(aca[1]));
  wo_mma2<L1, false, L1 ? L_TB : L_O2>(acc, smem, wrs, og + MB_, a, lane, wof, L1 ? 8 : 16, wp.r, qa, sent, [&](int s) __attribute__((always_inline)) {
    if (!L1 && s < 0) out_put(aca, aoa);
    if (!L1 && s == 8) oa = *(const LAS u32x4*)(size_t)xsa;
    if (!L1 && s == OSA) out_st(oa, soa, out);
    if (!L1 && s == OSA + 2) asm volatile("" :: "v"(oa));
  });
  if (!L1) {
    out_put(acc, wp.ao);
    lds_barrier();
    out_st(*(const LAS u32x4*)(size_t)wp.xs, wp.so, out);
  } else wo_epi<L1>(acc, wp, smem, wrs, ws, out, ONES);
  ST(!L1, 20); if (L1) ST(1, 1);
}

#undef DROLE_
#define DROLE_ 1
#define MMA(d_, a_, b_) asm volatile("v_mfma_f32_16x16x32_bf16 %0, %1, %2, %0" : "+v"(d_) : "v"(a_), "v"(b_))
__device__ __forceinline__ void mma_(f32x4& d, const bf16x8& a, const bf16x8& b) { MMA(d, a, b); }
template <int I, bool Z = false>
__device__ __forceinline__ void mma_w(f32x4& d, const bf16x8& w, const bf16x8& x) {
  if constexpr (I == 0 && !Z) asm volatile("v_mfma_f32_16x16x32_bf16 %0, %1, %2, %0" : "+v"(d) : "{v[76:79]}"(w), "v"(x));
  if constexpr (I == 0 && Z) asm volatile("v_mfma_f32_16x16x32_bf16 %0, %1, %2, 0" : "=&v"(d) : "{v[76:79]}"(w), "v"(x));
  if constexpr (I == 1 && Z) asm volatile("v_mfma_f32_16x16x32_bf16 %0, %1, %2, 0" : "=&v"(d) : "{v[80:83]}"(w), "v"(x));
  if constexpr (I == 2 && Z) asm volatile("v_mfma_f32_16x16x32_bf16 %0, %2, %1, 0" : "=&v"(d) : "{v[84:87]}"(w), "v"(x));
  if constexpr (I == 1 && !Z) asm volatile("v_mfma_f32_16x16x32_bf16 %0, %1, %2, %0" : "+v"(d) : "{v[80:83]}"(w), "v"(x));
  if constexpr (I == 2 && !Z) asm volatile("v_mfma_f32_16x16x32_bf16 %0, %2, %1, %0" : "+v"(d) : "{v[84:87]}"(w), "v"(x));
  if constexpr (I == 3) asm volatile("v_mfma_f32_16x16x32_bf16 %0, %1, %2, %0" : "+v"(d) : "{v[88:91]}"(w), "v"(x));
  if constexpr (I == 4) asm volatile("v_mfma_f32_16x16x32_bf16 %0, %1, %2, %0" : "+v"(d) : "{v[92:95]}"(w), "v"(x));
  if constexpr (I == 5) asm volatile("v_mfma_f32_16x16x32_bf16 %0, %2, %1, %0" : "+v"(d) : "{v[96:99]}"(w), "v"(x));
  if constexpr (I == 6) asm volatile("v_mfma_f32_16x16x32_bf16 %0, %1, %2, %0" : "+v"(d) : "{v[100:103]}"(w), "v"(x));
  if constexpr (I == 7) asm volatile("v_mfma_f32_16x16x32_bf16 %0, %1, %2, %0" : "+v"(d) : "{v[104:107]}"(w), "v"(x));
  if constexpr (I == 8) asm volatile("v_mfma_f32_16x16x32_bf16 %0, %2, %1, %0" : "+v"(d) : "{v[108:111]}"(w), "v"(x));
  if constexpr (I == 9) asm volatile("v_mfma_f32_16x16x32_bf16 %0, %1, %2, %0" : "+v"(d) : "{v[112:115]}"(w), "v"(x));
  if constexpr (I == 10) asm volatile("v_mfma_f32_16x16x32_bf16 %0, %1, %2, %0" : "+v"(d) : "{v[116:119]}"(w), "v"(x));
  if constexpr (I == 11) asm volatile("v_mfma_f32_16x16x32_bf16 %0, %2, %1, %0" : "+v"(d) : "{v[120:123]}"(w), "v"(x));
  if constexpr (I == 12) asm volatile("v_mfma_f32_16x16x32_bf16 %0, %1, %2, %0" : "+v"(d) : "{v[124:127]}"(w), "v"(x));
  if constexpr (I == 13) asm volatile("v_mfma_f32_16x16x32_bf16 %0, %1, %2, %0" : "+v"(d) : "{v[128:131]}"(w), "v"(x));
  if constexpr (I == 14) asm volatile("v_mfma_f32_16x16x32_bf16 %0, %2, %1, %0" : "+v"(d) : "{v[132:135]}"(w), "v"(x));
  if constexpr (I == 15) asm volatile("v_mfma_f32_16x16x32_bf16 %0, %1, %2, %0" : "+v"(d) : "{v[136:139]}"(w), "v"(x));
  if constexpr (I == 16) asm volatile("v_mfma_f32_16x16x32_bf16 %0, %1, %2, %0" : "+v"(d) : "{v[140:143]}"(w), "v"(x));
  if constexpr (I == 17) asm volatile("v_mfma_f32_16x16x32_bf16 %0, %2, %1, %0" : "+v"(d) : "{v[144:147]}"(w), "v"(x));
  if constexpr (I == 18) asm volatile("v_mfma_f32_16x16x32_bf16 %0, %1, %2, %0" : "+v"(d) : "{v[148:151]}"(w), "v"(x));
  if constexpr (I == 19) asm volatile("v_mfma_f32_16x16x32_bf16 %0, %1, %2, %0" : "+v"(d) : "{v[152:155]}"(w), "v"(x));
  if constexpr (I == 20) asm volatile("v_mfma_f32_16x16x32_bf16 %0, %2, %1, %0" : "+v"(d) : "{v[156:159]}"(w), "v"(x));
  if constexpr (I == 21) asm volatile("v_mfma_f32_16x16x32_bf16 %0, %1, %2, %0" : "+v"(d) : "{v[160:163]}"(w), "v"(x));
  if constexpr (I == 22) asm volatile("v_mfma_f32_16x16x32_bf16 %0, %1, %2, %0" : "+v"(d) : "{v[164:167]}"(w), "v"(x));
  if constexpr (I == 23) asm volatile("v_mfma_f32_16x16x32_bf16 %0, %2, %1, %0" : "+v"(d) : "{v[168:171]}"(w), "v"(x));
  if constexpr (I == 24) asm volatile("v_mfma_f32_16x16x32_bf16 %0, %1, %2, %0" : "+v"(d) : "{v[172:175]}"(w), "v"(x));
  if constexpr (I == 25) asm volatile("v_mfma_f32_16x16x32_bf16 %0, %1, %2, %0" : "+v"(d) : "{v[176:179]}"(w), "v"(x));
  if constexpr (I == 26) asm volatile("v_mfma_f32_16x16x32_bf16 %0, %2, %1, %0" : "+v"(d) : "{v[180:183]}"(w), "v"(x));
  if constexpr (I == 27) asm volatile("v_mfma_f32_16x16x32_bf16 %0, %1, %2, %0" : "+v"(d) : "{v[184:187]}"(w), "v"(x));
  if constexpr (I == 28) asm volatile("v_mfma_f32_16x16x32_bf16 %0, %1, %2, %0" : "+v"(d) : "{v[188:191]}"(w), "v"(x));
  if constexpr (I == 29) asm volatile("v_mfma_f32_16x16x32_bf16 %0, %2, %1, %0" : "+v"(d) : "{v[192:195]}"(w), "v"(x));
  if constexpr (I == 30) asm volatile("v_mfma_f32_16x16x32_bf16 %0, %1, %2, %0" : "+v"(d) : "{v[196:199]}"(w), "v"(x));
  if constexpr (I == 31) asm volatile("v_mfma_f32_16x16x32_bf16 %0, %1, %2, %0" : "+v"(d) : "{v[200:203]}"(w), "v"(x));
  if constexpr (I == 32) asm volatile("v_mfma_f32_16x16x32_bf16 %0, %2, %1, %0" : "+v"(d) : "{v[204:207]}"(w), "v"(x));
  if constexpr (I == 33) asm volatile("v_mfma_f32_16x16x32_bf16 %0, %1, %2, %0" : "+v"(d) : "{v[208:211]}"(w), "v"(x));
  if constexpr (I == 34) asm volatile("v_mfma_f32_16x16x32_bf16 %0, %1, %2, %0" : "+v"(d) : "{v[212:215]}"(w), "v"(x));
  if constexpr (I == 35) asm volatile("v_mfma_f32_16x16x32_bf16 %0, %2, %1, %0" : "+v"(d) : "{v[216:219]}"(w), "v"(x));
  if constexpr (I == 36) asm volatile("v_mfma_f32_16x16x32_bf16 %0, %1, %2, %0" : "+v"(d) : "{v[220:223]}"(w), "v"(x));
  if constexpr (I == 37) asm volatile("v_mfma_f32_16x16x32_bf16 %0, %1, %2, %0" : "+v"(d) : "{v[224:227]}"(w), "v"(x));
  if constexpr (I == 38) asm volatile("v_mfma_f32_16x16x32_bf16 %0, %2, %1, %0" : "+v"(d) : "{v[228:231]}"(w), "v"(x));
  if constexpr (I == 39) asm volatile("v_mfma_f32_16x16x32_bf16 %0, %1, %2, %0" : "+v"(d) : "{v[232:235]}"(w), "v"(x));
  if constexpr (I == 40) asm volatile("v_mfma_f32_16x16x32_bf16 %0, %1, %2, %0" : "+v"(d) : "{v[236:239]}"(w), "v"(x));
  if constexpr (I == 41) asm volatile("v_mfma_f32_16x16x32_bf16 %0, %2, %1, %0" : "+v"(d) : "{v[240:243]}"(w), "v"(x));
  if constexpr (I == 42) asm volatile("v_mfma_f32_16x16x32_bf16 %0, %1, %2, %0" : "+v"(d) : "{v[244:247]}"(w), "v"(x));
  if constexpr (I == 43) asm volatile("v_mfma_f32_16x16x32_bf16 %0, %1, %2, %0" : "+v"(d) : "{v[248:251]}"(w), "v"(x));
  if constexpr (I == 44) asm volatile("v_mfma_f32_16x16x32_bf16 %0, %2, %1, %0" : "+v"(d) : "{v[252:255]}"(w), "v"(x));
}
template <int I>
__device__ __forceinline__ void sum_w(f32x4& d, const u32x4& a, u32 ix, const bf16x8& w0, const bf16x8& w1) {
  if constexpr (I == 1) asm volatile("v_smfmac_f32_16x16x64_bf16 %0, %1, v[80:87], %2" : "+v"(d) : "v"(a), "v"(ix), "{v[80:83]}"(w0), "{v[84:87]}"(w1));
  if constexpr (I == 3) asm volatile("v_smfmac_f32_16x16x64_bf16 %0, %1, v[88:95], %2" : "+v"(d) : "v"(a), "v"(ix), "{v[88:91]}"(w0), "{v[92:95]}"(w1));
  if constexpr (I == 5) asm volatile("v_smfmac_f32_16x16x64_bf16 %0, %1, v[96:103], %2" : "+v"(d) : "v"(a), "v"(ix), "{v[96:99]}"(w0), "{v[100:103]}"(w1));
  if constexpr (I == 7) asm volatile("v_smfmac_f32_16x16x64_bf16 %0, %1, v[104:111], %2" : "+v"(d) : "v"(a), "v"(ix), "{v[104:107]}"(w0), "{v[108:111]}"(w1));
  if constexpr (I == 9) asm volatile("v_smfmac_f32_16x16x64_bf16 %0, %1, v[112:119], %2" : "+v"(d) : "v"(a), "v"(ix), "{v[112:115]}"(w0), "{v[116:119]}"(w1));
  if constexpr (I == 11) asm volatile("v_smfmac_f32_16x16x64_bf16 %0, %1, v[120:127], %2" : "+v"(d) : "v"(a), "v"(ix), "{v[120:123]}"(w0), "{v[124:127]}"(w1));
  if constexpr (I == 13) asm volatile("v_smfmac_f32_16x16x64_bf16 %0, %1, v[128:135], %2" : "+v"(d) : "v"(a), "v"(ix), "{v[128:131]}"(w0), "{v[132:135]}"(w1));
  if constexpr (I == 15) asm volatile("v_smfmac_f32_16x16x64_bf16 %0, %1, v[136:143], %2" : "+v"(d) : "v"(a), "v"(ix), "{v[136:139]}"(w0), "{v[140:143]}"(w1));
  if constexpr (I == 17) asm volatile("v_smfmac_f32_16x16x64_bf16 %0, %1, v[144:151], %2" : "+v"(d) : "v"(a), "v"(ix), "{v[144:147]}"(w0), "{v[148:151]}"(w1));
  if constexpr (I == 19) asm volatile("v_smfmac_f32_16x16x64_bf16 %0, %1, v[152:159], %2" : "+v"(d) : "v"(a), "v"(ix), "{v[152:155]}"(w0), "{v[156:159]}"(w1));
  if constexpr (I == 21) asm volatile("v_smfmac_f32_16x16x64_bf16 %0, %1, v[160:167], %2" : "+v"(d) : "v"(a), "v"(ix), "{v[160:163]}"(w0), "{v[164:167]}"(w1));
  if constexpr (I == 23) asm volatile("v_smfmac_f32_16x16x64_bf16 %0, %1, v[168:175], %2" : "+v"(d) : "v"(a), "v"(ix), "{v[168:171]}"(w0), "{v[172:175]}"(w1));
  if constexpr (I == 25) asm volatile("v_smfmac_f32_16x16x64_bf16 %0, %1, v[176:183], %2" : "+v"(d) : "v"(a), "v"(ix), "{v[176:179]}"(w0), "{v[180:183]}"(w1));
  if constexpr (I == 27) asm volatile("v_smfmac_f32_16x16x64_bf16 %0, %1, v[184:191], %2" : "+v"(d) : "v"(a), "v"(ix), "{v[184:187]}"(w0), "{v[188:191]}"(w1));
  if constexpr (I == 29) asm volatile("v_smfmac_f32_16x16x64_bf16 %0, %1, v[192:199], %2" : "+v"(d) : "v"(a), "v"(ix), "{v[192:195]}"(w0), "{v[196:199]}"(w1));
  if constexpr (I == 31) asm volatile("v_smfmac_f32_16x16x64_bf16 %0, %1, v[200:207], %2" : "+v"(d) : "v"(a), "v"(ix), "{v[200:203]}"(w0), "{v[204:207]}"(w1));
  if constexpr (I == 33) asm volatile("v_smfmac_f32_16x16x64_bf16 %0, %1, v[208:215], %2" : "+v"(d) : "v"(a), "v"(ix), "{v[208:211]}"(w0), "{v[212:215]}"(w1));
  if constexpr (I == 35) asm volatile("v_smfmac_f32_16x16x64_bf16 %0, %1, v[216:223], %2" : "+v"(d) : "v"(a), "v"(ix), "{v[216:219]}"(w0), "{v[220:223]}"(w1));
  if constexpr (I == 37) asm volatile("v_smfmac_f32_16x16x64_bf16 %0, %1, v[224:231], %2" : "+v"(d) : "v"(a), "v"(ix), "{v[224:227]}"(w0), "{v[228:231]}"(w1));
  if constexpr (I == 39) asm volatile("v_smfmac_f32_16x16x64_bf16 %0, %1, v[232:239], %2" : "+v"(d) : "v"(a), "v"(ix), "{v[232:235]}"(w0), "{v[236:239]}"(w1));
  if constexpr (I == 41) asm volatile("v_smfmac_f32_16x16x64_bf16 %0, %1, v[240:247], %2" : "+v"(d) : "v"(a), "v"(ix), "{v[240:243]}"(w0), "{v[244:247]}"(w1));
  if constexpr (I == 43) asm volatile("v_smfmac_f32_16x16x64_bf16 %0, %1, v[248:255], %2" : "+v"(d) : "v"(a), "v"(ix), "{v[248:251]}"(w0), "{v[252:255]}"(w1));
  if constexpr (I == 45) asm volatile("v_smfmac_f32_16x16x64_bf16 %0, %1, v[64:71], %2" : "+v"(d) : "v"(a), "v"(ix), "{v[64:67]}"(w0), "{v[68:71]}"(w1));
  if constexpr (I == 47) asm volatile("v_smfmac_f32_16x16x64_bf16 %0, %1, v[72:79], %2" : "+v"(d) : "v"(a), "v"(ix), "{v[72:75]}"(w0), "{v[76:79]}"(w1));
}
#define WREGS(X) \
  X(0, 76, 77, 78, 79) X(1, 80, 81, 82, 83) X(2, 84, 85, 86, 87) X(3, 88, 89, 90, 91) X(4, 92, 93, 94, 95) X(5, 96, 97, 98, 99) \
  X(6, 100, 101, 102, 103) X(7, 104, 105, 106, 107) X(8, 108, 109, 110, 111) X(9, 112, 113, 114, 115) X(10, 116, 117, 118, 119) X(11, 120, 121, 122, 123) \
  X(12, 124, 125, 126, 127) X(13, 128, 129, 130, 131) X(14, 132, 133, 134, 135) X(15, 136, 137, 138, 139) X(16, 140, 141, 142, 143) X(17, 144, 145, 146, 147) \
  X(18, 148, 149, 150, 151) X(19, 152, 153, 154, 155) X(20, 156, 157, 158, 159) X(21, 160, 161, 162, 163) X(22, 164, 165, 166, 167) X(23, 168, 169, 170, 171) \
  X(24, 172, 173, 174, 175) X(25, 176, 177, 178, 179) X(26, 180, 181, 182, 183) X(27, 184, 185, 186, 187) X(28, 188, 189, 190, 191) X(29, 192, 193, 194, 195) \
  X(30, 196, 197, 198, 199) X(31, 200, 201, 202, 203) X(32, 204, 205, 206, 207) X(33, 208, 209, 210, 211) X(34, 212, 213, 214, 215) X(35, 216, 217, 218, 219) \
  X(36, 220, 221, 222, 223) X(37, 224, 225, 226, 227) X(38, 228, 229, 230, 231) X(39, 232, 233, 234, 235) X(40, 236, 237, 238, 239) X(41, 240, 241, 242, 243) \
  X(42, 244, 245, 246, 247) X(43, 248, 249, 250, 251) X(44, 252, 253, 254, 255) X(45, 64, 65, 66, 67) X(46, 68, 69, 70, 71) X(47, 72, 73, 74, 75)
__device__ __forceinline__ void w_dma(rsrc_t r, int voff, int soff, int lds) {
  asm volatile("s_mov_b32 m0, %3\n\ts_nop 0\n\tbuffer_load_dwordx4 %0, %1, %2 offen lds\n\t"
               "s_add_u32 m0, m0, 3072\n\ts_nop 0\n\tbuffer_load_dwordx4 %0, %1, %2 offen offset:1024 lds\n\t"
               "s_add_u32 m0, m0, 3072\n\ts_nop 0\n\tbuffer_load_dwordx4 %0, %1, %2 offen offset:2048 lds"
               :: "v"(voff), "s"(r), "s"(soff), "s"(lds) : "memory", "scc");
}
template <int I>
__device__ __forceinline__ void w_ld(bf16x8& r, int vo, rsrc_t rs) {
#define X(n_, a_, b_, c_, d_) if constexpr (I == n_) asm volatile("buffer_load_dwordx4 %0, %1, %2, %3 offen offset:%4 sc1" : "={v[" #a_ ":" #d_ "]}"(r) : "v"(vo), "s"(rs), "s"(OFF_WQ + (I / 3) * 98304), "n"((I % 3) * 1024) : "memory");
  WREGS(X)
#undef X
}
template <int I, int O>
__device__ __forceinline__ void w_get(bf16x8& r, u32 a) {
#define X(n_, a_, b_, c_, d_) if constexpr (I == n_) asm volatile("ds_read_b64_tr_b16 v[" #a_ ":" #b_ "], %1 offset:%2\n\tds_read_b64_tr_b16 v[" #c_ ":" #d_ "], %1 offset:%3" : "=&{v[" #a_ ":" #d_ "]}"(r) : "v"(a), "n"(O), "n"(O + 2048) : "memory");
  WREGS(X)
#undef X
}
__device__ __forceinline__ float vmuln_(float a, float b) { float d; asm volatile("v_mul_f32_e64 %0, -%1, %2" : "=v"(d) : "v"(a), "v"(b)); return d; }
__device__ __forceinline__ float vmul_(float a, float b) { float d; asm volatile("v_mul_f32 %0, %1, %2" : "=v"(d) : "v"(a), "v"(b)); return d; }
__device__ __forceinline__ float vfman_(float c, float a, float b) { float d; asm volatile("v_fma_f32 %0, -%1, %2, %3" : "=v"(d) : "v"(a), "v"(b), "v"(c)); return d; }
__device__ __forceinline__ float vfma_(float a, float b, float c) { float d; asm volatile("v_fma_f32 %0, %1, %2, %3" : "=v"(d) : "v"(a), "v"(b), "v"(c)); return d; }
__device__ __forceinline__ u32 vcvt_(float a, float b) { u32 d; asm volatile("v_cvt_pk_bf16_f32 %0, %1, %2" : "=v"(d) : "v"(a), "v"(b)); return d; }
__device__ __forceinline__ float vsel0_(float a, u64 m) { float d; asm volatile("v_cndmask_b32_e64 %0, %1, 0, %2" : "=v"(d) : "v"(a), "s"(m)); return d; }
__device__ __forceinline__ u32 vlsa_(u32 a, int b) { u32 d; asm volatile("v_lshl_add_u32 %0, %1, 1, %2" : "=v"(d) : "v"(a), "s"(b)); return d; }
__device__ __forceinline__ float vmov_(u32 a) { float d; asm volatile("v_mov_b32 %0, %1" : "=v"(d) : "s"(a)); return d; }
__device__ __forceinline__ float vexp_(float a) { float d; asm volatile("v_exp_f32 %0, %1" : "=v"(d) : "v"(a)); return d; }
__device__ __forceinline__ float vrcp_(float a) { float d; asm volatile("v_rcp_f32 %0, %1" : "=v"(d) : "v"(a)); return d; }
__device__ __forceinline__ float vadd_(float a, float b) { float d; asm volatile("v_add_f32 %0, %1, %2" : "=v"(d) : "v"(a), "v"(b)); return d; }
__device__ __forceinline__ float vmax_(float a, float b) { float d; asm volatile("v_max_f32 %0, %1, %2" : "=v"(d) : "v"(a), "v"(b)); return d; }
__device__ __forceinline__ float vmax3_(float a, float b, float c) { float d; asm volatile("v_max3_f32 %0, %1, %2, %3" : "=v"(d) : "v"(a), "v"(b), "v"(c)); return d; }
__device__ __forceinline__ u32 vperm_(u32 a, u32 sl) { u32 d; asm volatile("v_perm_b32 %0, %1, %1, %2" : "=v"(d) : "v"(a), "v"(sl)); return d; }
__device__ __forceinline__ void vsw16_(float& a, float& b) { asm volatile("v_permlane16_swap_b32 %0, %1" : "+v"(a), "+v"(b)); }
__device__ __forceinline__ void vsw32_(float& a, float& b) { asm volatile("v_permlane32_swap_b32 %0, %1" : "+v"(a), "+v"(b)); }
__device__ __forceinline__ void mm16z_(f32x4& d, u32x2 a, u32x2 b) { asm volatile("v_mfma_f32_16x16x16_bf16 %0, %1, %2, 0" : "=&v"(d) : "v"(a), "v"(b)); }
#define NOP(n_) asm volatile("s_nop %0" :: "n"(n_))
__device__ __forceinline__ void touch_(bf16x4& a, bf16x4& b) { asm volatile("" : "+v"(a), "+v"(b)); }
__device__ __forceinline__ void vfmac_(float& d, float a, float b) { asm volatile("v_fmac_f32 %0, %1, %2" : "+v"(d) : "v"(a), "v"(b)); }
__device__ __forceinline__ u32 vadds_(u32 a, int b) { u32 d; asm volatile("v_add_u32 %0, %2, %1" : "=v"(d) : "v"(a), "s"(b)); return d; }
#define LD4(p_, o_) (*(const LAS f32x4*)((p_) + (o_)))
#define LD1(p_, o_) (*(const LAS float*)((p_) + (o_)))
#define LD2(p_, o_, d_) (f32x2){LD1(p_, o_), LD1(p_, (o_) + (d_))}
#define IN(o_, b_, c_) ((o_) >= (b_) && (o_) < (b_) + (c_))
struct GPre { u32 ap, fr, fq, fw, sp, l16, sl; };
struct GSum { f32x4 cs[2]; u32x4 on[3]; u32 ix; };
template <int I>
__device__ __forceinline__ void g_sum(GSum& S, bf16x8 (&wf)[48]) { sum_w<2 * I + 1>(S.cs[I & 1], S.on[(2 * I + 1) % 3], S.ix, wf[2 * I + 1], wf[(2 * I + 2) % 48]); }
__device__ __forceinline__ void g_pre(GPre& P, int g, bool pb) {
  const int lane = lane_id();
  P.ap = pb ? (L_TA + lane * 8 + (lane & 4) * 4096) ^ 32 : L_TA + lane * 8; P.fr = L_FS + g * 768 + (lane & 15) * 4; P.fq = L_FS + g * 768 + (lane >> 4) * 16; P.fw = L_FS + g * 768 + lane * 4;
  P.sp = L_SP + min(lane, 47) * 16; P.l16 = lane * 16;
  asm volatile("" : "+v"(P.ap), "+v"(P.fr), "+v"(P.fq), "+v"(P.fw), "+v"(P.sp), "+v"(P.l16));
}
#define SPIN(x_) asm volatile("" : "+s"(x_))
template <int LY, int H>
__device__ __forceinline__ void g_gemm(char* smem, rsrc_t wrs, int mo, int hso, int oo, u32 tag, int g, GPre& P, GSum& S, bf16x8 (&wf)[48]) {
  const LAS char *fr = (const LAS char*)(size_t)P.fr, *fq = (const LAS char*)(size_t)P.fq;
  constexpr int cso = 512;
  const LAS f32x4* sp = (const LAS f32x4*)(size_t)P.sp + 96 * H;
  if (H) PAGEV(10 + LY);
  if (LY || H) lds_barrier();
  else asm volatile("s_barrier" ::: "memory");
  ST(LY, 11 + 2 * H);
  u32x4 r[3], on;
  HaloMsg hm;
  GCst K;
  u32 su, ni, ao, u0, u1;
  bf16x4 xl[32], xh[32];
  bf16x8 wx[3];
  f32x4 aK[2], aV[2], aQ[2], s1, s2, cK, cQ, nv, rv;
  f32x2 rn;
  u32x4 mk, mv, q0, q1;
  float t0, t1, t2, t3, cV, tk[4], tv[4], tq[4];
#define OWS(s_) ((s_) < 16 ? 12 : OP2)
#define OKS(s_) (((s_) & 15) < OWS(s_) ? (s_) & 15 : ((s_) & 15) == OWS(s_) ? 15 : ((s_) & 15) - 1)
#define OLDL(s_) xl[s_] = __builtin_amdgcn_ds_read_tr16_b64_v4i16((LAS bf16x4*)((const LAS char*)(size_t)P.ap + (LY && (s_) < 16 ? 16384 : 0) + H * (L_TB - L_TA) + OKS(s_) * 1024))
#define OLDH(s_) xh[s_] = __builtin_amdgcn_ds_read_tr16_b64_v4i16((LAS bf16x4*)((const LAS char*)(size_t)P.ap + (LY && (s_) < 16 ? 16384 : 0) + H * (L_TB - L_TA) + OKS(s_) * 1024 + 512))
#pragma unroll
  for (int s = 0; s < 2; ++s) { OLDL(s); OLDH(s); }
  s1 = sp[0]; s2 = sp[48];
  SB();
#pragma unroll
  for (int s = 2; s < GPF; ++s) { OLDL(s); OLDH(s); }
  SB();
  const u32 qp = vadds_(P.l16, L_QB + g * 3072);
  const int ma = (int)vadds_(P.l16, mo);
  constexpr bool EX = LY ? EX2 : EX1;
  if (!EX) asm volatile("v_alignbit_b32 %0, %2, %2, %1" : "=v"(P.sl) : "v"(P.l16), "s"(0x0c0c0100u));
  if (H) {
    K.PAR = 0xAAAAAAAAAAAAAAAAull; K.VM[0] = 0x0fff00ff000f0000ull; K.VM[1] = 0x3fff03ff003f0003ull; K.VM[2] = 0xf000ff00fff0ffffull; K.VM[3] = 0xc000fc00ffc0fffcull; K.sc = 0x3eb8aa3bu;
    SPIN(K.PAR); SPIN(K.VM[0]); SPIN(K.VM[1]); SPIN(K.VM[2]); SPIN(K.VM[3]); SPIN(K.sc); SPIN(oo); SPIN(hso);
  }
  const float eps = vmov_(0x3727c5acu);
  auto slot = [&](auto N_) __attribute__((always_inline)) {
    constexpr int n = decltype(N_)::value, s = n / 3, j = n % 3, p = s >> 4, ks = OKS(s), m = n % 48;
    SB();
    if constexpr (j == 0 && !(s & 1)) touch_(xl[s + 1], xh[s + 1]);
    {
      const bf16x8 x = __builtin_shufflevector(xl[s], xh[s], 0, 1, 2, 3, 4, 5, 6, 7);
      if constexpr (ks == 15 && j == 0) mma_(aK[p], wx[0], x);
      else if constexpr (ks == 15 && j == 1) mma_(aV[p], x, wx[1]);
      else if constexpr (ks == 15 && j == 2) mma_(aQ[p], wx[2], x);
      else mma_w<3 * ks + (j + 1) % 3, ks == 0>(j == 0 ? aK[p] : j == 1 ? aV[p] : aQ[p], wf[3 * ks + (j + 1) % 3], x);
    }
    SB();
    if constexpr (j == 0 && s + GPF < 32) OLDL(s + GPF);
    if constexpr (j == 1 && s + GPF < 32) OLDH(s + GPF);
    SB();
    constexpr int wm = m - (3 * OWS(s) - 8);
    if constexpr (IN(wm, 0, 3)) wx[wm] = *(const LAS bf16x8*)(size_t)(qp + (L_WX - L_QB) + ((wm + 1) % 3) * 1024);
    if constexpr (n == 6) t0 = vadd_(s1[0], s1[1]);
    if constexpr (n == 7) t1 = vadd_(s1[2], s1[3]);
    if constexpr (n == 8) { t2 = vadd_(s2[0], s2[1]); t3 = vadd_(s2[2], s2[3]); }
    if constexpr (n == 9) t0 = vadd_(t0, t1);
    if constexpr (n == 10) t2 = vadd_(t2, t3);
    if constexpr (n == 11) { t0 *= (1.f / 512.f); PIN(t0); }
    if constexpr (n == 12) t1 = vfman_(eps, t0, t0);
    if constexpr (n == 13) { t1 = fmaf(t2, 1.f / 512.f, t1); PIN(t1); }
    if constexpr (n == 14) t1 = vmax_(t1, eps);
    if constexpr (n == 15) { t1 = __builtin_amdgcn_rsqf(t1); PIN(t1); }
    if constexpr (n == 16) t0 = vmuln_(t0, t1);
    if constexpr (n == 18) { LAS float* fw = (LAS float*)(size_t)P.fw; fw[0] = t1; fw[64] = t0; asm volatile("" ::: "memory"); }
    if constexpr (LY == 0 && H == 0) {
      if constexpr (n == 17) { asm volatile("s_waitcnt vmcnt(6)" ::: "memory"); g_sum<17>(S, wf); g_sum<18>(S, wf); }
      if constexpr (n == 20) { asm volatile("s_waitcnt vmcnt(3)" ::: "memory"); g_sum<19>(S, wf); }
      if constexpr (n == 23) { asm volatile("s_waitcnt vmcnt(0)" ::: "memory"); g_sum<20>(S, wf); g_sum<21>(S, wf); }
      if constexpr (n == 24) t2 = vadd_(S.cs[0][0], S.cs[0][1]);
      if constexpr (n == 25) { t3 = vadd_(S.cs[1][0], S.cs[1][1]); asm volatile("" :: "v"(S.cs[0]), "v"(S.cs[1])); }
      if constexpr (n == 26) t2 = vadd_(t2, t3);
      if constexpr (n == 27) { ((LAS float*)(size_t)P.fw)[128] = t2; asm volatile("" ::: "memory"); }
    }
    if constexpr (n == 31) cK = LD4(fq, cso + 64);
    if constexpr (n == 32) { rn = LD2(fr, 64, 256); cV = LD1(fr, cso + 128); }
    if constexpr (n == 33) nv = LD4(fq, 256 + 64);
    if constexpr (n == 34) rv = LD4(fq, 64);
    if constexpr (n == 37) mk[1] = __float_as_uint(vmov_(tag));
    if constexpr (n == 38) { tk[0] = vmul_(cK[0], rn[1]); tk[1] = vmul_(cK[1], rn[1]); }
    if constexpr (IN(n, 39, 2)) tk[n - 37] = vmul_(cK[n - 37], rn[1]);
    if constexpr (IN(n, 41, 4)) tv[n - 41] = vmul_(nv[n - 41], cV);
    if constexpr (n == 44) mk[3] = __float_as_uint(vmov_(tag));
    if constexpr (IN(n, 45, 2)) mv[2 * (n - 45) + 1] = __float_as_uint(vmov_(tag));
    if constexpr (n == 47) { vfmac_(tk[0], aK[0][0], rn[0]); vfmac_(tk[1], aK[0][1], rn[0]); vfmac_(tk[2], aK[0][2], rn[0]); }
    if constexpr (n == 48) { vfmac_(tk[3], aK[0][3], rn[0]); mk[0] = vcvt_(tk[0], tk[1]); vfmac_(tv[0], aV[0][0], rv[0]); }
    if constexpr (n == 49) { mk[2] = vcvt_(tk[2], tk[3]); vfmac_(tv[1], aV[0][1], rv[1]); stw(wrs, ma, mk); }
    if constexpr (n == 50) { vfmac_(tv[2], aV[0][2], rv[2]); vfmac_(tv[3], aV[0][3], rv[3]); mv[0] = vcvt_(tv[0], tv[1]); mv[2] = vcvt_(tv[2], tv[3]); }
    if constexpr (n == (LY ? KV2 : KV1)) stw(wrs, ma + 1024, mv);
    if constexpr (n == 53) cQ = LD4(fq, cso);
    if constexpr (n == KWN && !H) asm volatile("ds_write2_b32 %0, %1, %2 offset1:1" :: "v"(qp), "v"(mk[0]), "v"(mk[2]), "v"(mk) : "memory");
    if constexpr (n == KWN + 1 && !H) asm volatile("ds_write2_b32 %0, %1, %2 offset0:2 offset1:3" :: "v"(qp), "v"(mv[0]), "v"(mv[2]), "v"(mv) : "memory");
    if constexpr (IN(n, 59, 4)) tq[n - 59] = vmul_(cQ[n - 59], rn[1]);
    if constexpr (IN(n, 63, 4)) vfmac_(tq[n - 63], aQ[0][n - 63], rn[0]);
    if constexpr (IN(n, 67, 2)) q0[n - 67] = vcvt_(tq[2 * (n - 67)], tq[2 * (n - 67) + 1]);
    if constexpr (n == 69) rn = LD2(fr, 0, 256);
    if constexpr (n == 70) cK = LD4(fq, cso + 64);
    if constexpr (n == 72) nv = LD4(fq, 256);
    if constexpr (IN(n, 79, 4)) tq[n - 79] = vmul_(cQ[n - 79], rn[1]);
    if constexpr (n == 83) rv = LD4(fq, 0);
    if constexpr (IN(n, 84, 2)) { tk[2 * n - 168] = vmul_(cK[2 * n - 168], rn[1]); tk[2 * n - 167] = vmul_(cK[2 * n - 167], rn[1]); }
    if constexpr (IN(n, 86, 2)) { tv[2 * n - 172] = vmul_(nv[2 * n - 172], cV); tv[2 * n - 171] = vmul_(nv[2 * n - 171], cV); }
    static_assert(GPF == 4 && L_TA % 512 == 0, "");
    if constexpr (n == 35 && !LY) asm volatile("v_xor_b32 %0, 32, %0" : "+v"(P.ap));
    if constexpr (n == (LY ? PA2 : PA1)) __builtin_amdgcn_s_setprio(1);
    if constexpr (n == (LY ? PB2 : PB1)) __builtin_amdgcn_s_setprio(0);
    if constexpr (n == 90 && !LY && !H && EX) asm volatile("v_xor_b32 %0, 32, %0" : "+v"(P.ap));
    if constexpr (n == 90 && !EX) asm volatile("v_lshrrev_b32 %0, 1, %1" : "=v"(P.ap) : "v"(qp));
    if constexpr (n == 91 && !EX) P.ap = vadds_(P.ap, L_TB - (L_QB + g * 3072) / 2);
    if constexpr (H) {
      if constexpr (IN(n, 54, 2)) asm volatile("v_mov_b32 %0, %1" : "=v"(r[0][n - 54]) : "v"(mk[2 * (n - 54)]), "v"(mk));
      if constexpr (IN(n, 56, 2)) asm volatile("v_mov_b32 %0, %1" : "=v"(r[0][n - 54]) : "v"(mv[2 * (n - 56)]), "v"(mv));
      if constexpr (n == (LY ? NH2 : NH1)) { hm.k = ldws(wrs, P.l16, hso); hm.v = ldws(wrs, P.l16 + 1024, hso); }
      if constexpr (n == 89) { asm volatile("v_and_b32 %0, 0x100, %1" : "=v"(ao) : "v"(P.l16)); asm volatile("v_and_b32 %0, 0xf0, %1" : "=v"(u0) : "v"(P.l16)); }
      if constexpr (n == 90) { asm volatile("v_lshrrev_b32 %0, 9, %1" : "=v"(u1) : "v"(P.l16)); asm volatile("v_lshl_add_u32 %0, %0, 2, %1" : "+v"(ao) : "s"(oo)); }
      if constexpr (n == 91) { asm volatile("v_lshl_add_u32 %0, %1, 8, %0" : "+v"(u0) : "v"(u1)); asm volatile("v_alignbit_b32 %0, %2, %2, %1" : "=v"(P.sl) : "v"(P.l16), "s"(0x0c0c0100u)); }
      if constexpr (n == 92) { asm volatile("v_add_u32 %0, %0, %1" : "+v"(ao) : "v"(u0)); asm volatile("v_pk_max_i16 %0, %1, %2" : "=v"(su) : "v"(P.sl), "s"(0x03020302u)); }
      if constexpr (IN(n, 93, 2)) { asm volatile("v_mov_b32 %0, 0x3f803f80" : "=v"(on[2 * n - 186])); asm volatile("v_mov_b32 %0, 0x3f803f80" : "=v"(on[2 * n - 185])); }
      if constexpr (n == 95) asm volatile("v_mov_b32 %0, 0xff800000" : "=v"(ni));
    }
    if constexpr (n == 92) PIN(rv);
  };
  static_for<0, 96>(slot);
  SB();
#pragma unroll
  for (int e = 0; e < 4; ++e) vfmac_(tk[e], aK[1][e], rn[0]);
#pragma unroll
  for (int e = 0; e < 4; ++e) vfmac_(tv[e], aV[1][e], rv[e]);
  q0[2] = vcvt_(tk[0], tk[1]); q0[3] = vcvt_(tk[2], tk[3]);
  vfmac_(tq[0], aQ[1][0], rn[0]);
  if (!H) *(LAS u32x4*)(size_t)(qp + 1024) = q0;
  SB();
#pragma unroll
  for (int e = 1; e < 4; ++e) vfmac_(tq[e], aQ[1][e], rn[0]);
  q1[0] = vcvt_(tv[0], tv[1]); q1[1] = vcvt_(tv[2], tv[3]); q1[2] = vcvt_(tq[0], tq[1]); q1[3] = vcvt_(tq[2], tq[3]);
  if (!H) {
    *(LAS u32x4*)(size_t)(qp + 2048) = q1;
    asm volatile("" :: "v"(q0), "v"(q1) : "memory");
  } else {
  }
}
#define KPD 10
template <int LY>
__device__ __forceinline__ void g_kv(char* smem, rsrc_t wrs, int mo, int hso, int oo, u32 tag, int g, GPre& P, bf16x8 (&wf)[48]) {
  const LAS char *fr = (const LAS char*)(size_t)P.fr, *fq = (const LAS char*)(size_t)P.fq;
  constexpr int NH = LY ? NH2 : NH1, PD = LY ? KPD : KP1, PR = LY ? GPR : GR1, PE = LY ? GPE : GE1, PT = LY ? 88 : GT1, cso = 512, W1 = 10, W2 = 5;
  const LAS f32x4* sp = (const LAS f32x4*)(size_t)P.sp + 96;
  PAGEV(10 + LY);
  lds_barrier();
  ST(LY, 13);
  u32x4 r[3], on, mk, mv;
  HaloMsg hm;
  GCst K;
  u32 sl, su, ni, ao, u1, ap0;
  bf16x4 xr[96];
  bf16x8 wx[3];
  f32x4 aK[2], aV[2], aQ[2], s1, s2, cK, cQ, nv, rv, tq0, tv;
  f32x2 rn, r0;
  float t0, t1, t2, t3, cV, tk[4], tq[4];
#define KKS(s_, w_) ((s_) < (w_) ? (s_) : (s_) == (w_) ? 15 : (s_) - 1)
#define KT0(f_) ((f_) >= 16 && ((f_) & 1))
#define KFO(f_) ((LY && !KT0(f_) ? 16384 : 0) + ((f_) < 16 ? KKS(f_, W1) : KKS(((f_) - 16) / 2, W2)) * 1024)
#define KLD(r_) xr[r_] = __builtin_amdgcn_ds_read_tr16_b64_v4i16((LAS bf16x4*)((const LAS char*)(size_t)(!LY && KT0((r_) / 2) ? ap0 : P.ap) + (L_TB - L_TA) + KFO((r_) / 2) + ((r_) & 1) * 512))
#pragma unroll
  for (int e = 0; e < 4; ++e) KLD(e);
  s1 = sp[0]; s2 = sp[48];
  SB();
#pragma unroll
  for (int e = 4; e < PD; ++e) KLD(e);
  SB();
  int wo_ = L_WX + g * 3072;
  SPIN(wo_); SPIN(mo);
  u32 wp = vadds_(P.l16, wo_);
  K.PAR = 0xAAAAAAAAAAAAAAAAull; K.VM[0] = 0x0fff00ff000f0000ull; K.VM[1] = 0x3fff03ff003f0003ull; K.VM[2] = 0xf000ff00fff0ffffull; K.VM[3] = 0xc000fc00ffc0fffcull; K.sc = 0x3eb8aa3bu;
  u32 c0 = 0x0c0c0100u, c1 = 0x03020302u, c2 = 0xf0u;
  SPIN(K.PAR); SPIN(K.VM[0]); SPIN(K.VM[1]); SPIN(K.VM[2]); SPIN(K.VM[3]); SPIN(K.sc); SPIN(oo); SPIN(hso); SPIN(c0); SPIN(c1); SPIN(c2);
  const float eps = vmov_(0x3727c5acu);
  mk[1] = __float_as_uint(vmov_(tag)); mk[3] = __float_as_uint(vmov_(tag)); mv[1] = __float_as_uint(vmov_(tag)); mv[3] = __float_as_uint(vmov_(tag));
  auto slot = [&](auto N_) __attribute__((always_inline)) {
    constexpr int n = decltype(N_)::value;
    constexpr int st = n < 32 ? n / 2 : n < 88 ? (n - 32) / 4 : 14 + (n < 92 ? (n - 88) / 2 : n & 1);
    constexpr int j = n < 32 ? 1 + (n & 1) : n < 88 ? n & 3 : n < 92 ? n & 1 : n < 94 ? 3 : 2;
    constexpr int f = n < 32 ? st : 16 + 2 * st + (j > 0), ks = KKS(st, n < 32 ? W1 : W2), p = n >= 32 && j, m = j % 3;
    SB();
    if constexpr (n % 4 == 0 && n < 92) touch_(xr[n + 2], xr[n + 3]);
    if constexpr (n == 90) touch_(xr[94], xr[95]);
    {
      const bf16x8 x = __builtin_shufflevector(xr[2 * f], xr[2 * f + 1], 0, 1, 2, 3, 4, 5, 6, 7);
      f32x4& d = m == 0 ? aQ[p] : m == 1 ? aK[p] : aV[p];
      if constexpr (ks == 15 && m == 2) mma_(d, x, wx[2]);
      else if constexpr (ks == 15) mma_(d, wx[m], x);
      else mma_w<3 * ks + m, ks == 0>(d, wf[3 * ks + m], x);
    }
    SB();
    if constexpr (n + PD < 96) KLD(n + PD);
    SB();
    if constexpr (IN(n, 13, 2)) wx[n - 12] = *(const LAS bf16x8*)(size_t)(wp + (n - 12) * 1024);
    if constexpr (IN(n, 44, 3)) wx[n - 44] = *(const LAS bf16x8*)(size_t)(wp + (n - 44) * 1024);
    if constexpr (n == 1) { t0 = vadd_(s1[0], s1[1]); t1 = vadd_(s1[2], s1[3]); }
    if constexpr (n == 2) { t2 = vadd_(s2[0], s2[1]); t3 = vadd_(s2[2], s2[3]); }
    if constexpr (n == 4) t0 = vadd_(t0, t1);
    if constexpr (n == 5) { t2 = vadd_(t2, t3); t0 *= (1.f / 512.f); PIN(t0); }
    if constexpr (n == 6) t1 = vfman_(eps, t0, t0);
    if constexpr (n == 7) { t1 = fmaf(t2, 1.f / 512.f, t1); PIN(t1); }
    if constexpr (n == 8) t1 = vmax_(t1, eps);
    if constexpr (n == 9) { t1 = __builtin_amdgcn_rsqf(t1); PIN(t1); }
    if constexpr (n == 10) t0 = vmuln_(t0, t1);
    if constexpr (n == 12) { LAS float* fw = (LAS float*)(size_t)P.fw; fw[0] = t1; fw[64] = t0; asm volatile("" ::: "memory"); }
    static_assert(34 - PD > 22, "");
    if constexpr (n == 22 && !LY) asm volatile("v_xor_b32 %0, 32, %1" : "=v"(ap0) : "v"(P.ap));
    if constexpr (n == 16) cK = LD4(fq, cso + 64);
    if constexpr (n == 17) rn = LD2(fr, 64, 256);
    if constexpr (n == 18) cV = LD1(fr, cso + 128);
    if constexpr (n == 20) nv = LD4(fq, 256 + 64);
    if constexpr (n == 21) rv = LD4(fq, 64);
    if constexpr (IN(n, 24, 3)) tk[n - 24] = vmul_(cK[n - 24], rn[1]);
    if constexpr (n == 26) tk[3] = vmul_(cK[3], rn[1]);
    if constexpr (IN(n, 28, 3)) tv[n - 28] = vmul_(nv[n - 28], cV);
    if constexpr (n == 30) tv[3] = vmul_(nv[3], cV);
#define FMV(e_) do { float y_ = tv[e_]; vfmac_(y_, aV[0][e_], rv[e_]); tv[e_] = y_; } while (0)
    if constexpr (n == 32) { vfmac_(tk[0], aK[0][0], rn[0]); vfmac_(tk[1], aK[0][1], rn[0]); vfmac_(tk[2], aK[0][2], rn[0]); }
    if constexpr (n == 33) { vfmac_(tk[3], aK[0][3], rn[0]); mk[0] = vcvt_(tk[0], tk[1]); FMV(0); }
    if constexpr (n == 34) { mk[2] = vcvt_(tk[2], tk[3]); FMV(1); __builtin_amdgcn_raw_buffer_store_b128(mk, wrs, P.l16, mo, 16); }
    if constexpr (n == 35) { FMV(2); FMV(3); mv[0] = vcvt_(tv[0], tv[1]); mv[2] = vcvt_(tv[2], tv[3]); }
    if constexpr (n == 36) __builtin_amdgcn_raw_buffer_store_b128(mv, wrs, P.l16 + 1024, mo, 16);
    if constexpr (IN(n, 37, 2)) asm volatile("v_mov_b32 %0, %1" : "=v"(r[0][n - 37]) : "v"(mk[2 * (n - 37)]), "v"(mk));
    if constexpr (IN(n, 40, 2)) asm volatile("v_mov_b32 %0, %1" : "=v"(r[0][n - 38]) : "v"(mv[2 * (n - 40)]), "v"(mv));
    if constexpr (n == 42) wp = vadds_(P.l16, wo_);
    if constexpr (n == 47) asm volatile("v_and_b32 %0, 0x100, %1" : "=v"(ao) : "v"(P.l16));
    if constexpr (n == 48) asm volatile("v_lshl_add_u32 %0, %0, 2, %1" : "+v"(ao) : "s"(oo));
    if constexpr (n == 49) asm volatile("v_and_or_b32 %0, %1, %2, %0" : "+v"(ao) : "v"(P.l16), "s"(c2));
    if constexpr (n == 50) asm volatile("v_lshrrev_b32 %0, 9, %1" : "=v"(u1) : "v"(P.l16));
    if constexpr (n == 52) asm volatile("v_lshl_add_u32 %0, %1, 8, %0" : "+v"(ao) : "v"(u1));
    if constexpr (n == 53) cQ = LD4(fq, cso);
    if constexpr (n == 54) r0 = LD2(fr, 0, 256);
    if constexpr (n == 56) cK = LD4(fq, cso + 64);
    if constexpr (n == 57) nv = LD4(fq, 256);
    if constexpr (n == 58) cV = *(const volatile LAS float*)(fr + cso + 128);
    constexpr int o = n >= 60 && n % 4 != 3 ? (n - 60) / 4 * 3 + n % 4 : -1;
    if constexpr (IN(o, 0, 4)) tq[o] = vmul_(cQ[o], rn[1]);
    if constexpr (IN(o, 4, 4)) tq0[o - 4] = vmul_(cQ[o - 4], r0[1]);
    if constexpr (IN(o, 8, 4)) tk[o - 8] = vmul_(cK[o - 8], r0[1]);
    if constexpr (IN(o, 12, 4)) tv[o - 12] = vmul_(nv[o - 12], cV);
    if constexpr (o == 16) asm volatile("v_alignbit_b32 %0, %2, %2, %1" : "=v"(sl) : "v"(P.l16), "s"(c0));
    if constexpr (o == 17) asm volatile("v_pk_max_i16 %0, %1, %2" : "=v"(su) : "v"(sl), "s"(c1));
    if constexpr (o == 18) asm volatile("v_mov_b32 %0, 0xff800000" : "=v"(ni));
    if constexpr (n == 90) rv = LD4(fq, 0);
    if constexpr (n == 90) asm volatile("v_mov_b32 %0, 0x3f803f80" : "=v"(on[0]));
    if constexpr (n == 91) { asm volatile("v_mov_b32 %0, 0x3f803f80" : "=v"(on[1])); asm volatile("v_mov_b32 %0, 0x3f803f80" : "=v"(on[2])); asm volatile("v_mov_b32 %0, 0x3f803f80" : "=v"(on[3])); }
    if constexpr (n == NH) { hm.k = ldws(wrs, P.l16, hso); hm.v = ldws(wrs, P.l16 + 1024, hso); }
    if constexpr (n == PR || n == PT) __builtin_amdgcn_s_setprio(1);
    if constexpr (n == PE) __builtin_amdgcn_s_setprio(0);
    if constexpr (n == NH + NSG && (LY || SG1)) cnt_add(smem, C_HR, 0);
    if constexpr (n == 92) { vfmac_(tq[0], aQ[0][0], rn[0]); vfmac_(tq[1], aQ[0][1], rn[0]); vfmac_(tq[2], aQ[0][2], rn[0]); }
    if constexpr (n == 93) { vfmac_(tq[3], aQ[0][3], rn[0]); r[1][0] = vcvt_(tq[0], tq[1]); vfmac_(tk[0], aK[1][0], r0[0]); }
    if constexpr (n == 94) { r[1][1] = vcvt_(tq[2], tq[3]); vfmac_(tk[1], aK[1][1], r0[0]); vfmac_(tk[2], aK[1][2], r0[0]); vfmac_(tk[3], aK[1][3], r0[0]); }
    if constexpr (n == 95) { r[1][2] = vcvt_(tk[0], tk[1]); r[1][3] = vcvt_(tk[2], tk[3]); }
  };
  static_for<0, 96>(slot);
  SB();
  ST(LY, 14);
  attention_g(r, hm, (int)P.l16, hso, tag, wrs, (int)ao, sl, su, ni, on, K, 20 + LY, aQ[1], aV[1], tq0, tv, rv, r0[0]);
  if (!LY) __builtin_amdgcn_s_setprio(0);
}
__device__ __forceinline__ void mmz_(f32x4& d, const u32x4& a, const u32x4& b) { asm volatile("v_mfma_f32_16x16x32_bf16 %0, %1, %2, 0" : "=&v"(d) : "v"(a), "v"(b)); }
constexpr int GN = 128, GW = 42;
constexpr int g_step(int n) { return n < 32 ? n / 2 : 16 + (n - 32) / 3; }
constexpr int g_pos(int n) { return n < 32 ? n & 1 : (n - 32) % 3; }
constexpr int g_rd(int n) { return g_pos(n) < 2 && g_step(n) + GPF < 48; }
constexpr int g_wt(int n) { return n < GN - 1 && g_pos(n + 1) == 0 && !(g_step(n + 1) & 1); }
struct GSched { int n, g[256], k[256], pos[256], first[GN + 2]; };
constexpr GSched g_sched() {
  GSched S{};
  int n = 0, used = 0;
  auto add = [&](int g, int k0, int k1, int mn = 0, int lat = 0, int w = 1, int each = 0) {
    for (int k = k0; k < k1; ++k) {
      int t = S.n ? S.pos[S.n - 1] + (k == k0 ? lat : each) : 0;
      if (t < mn) t = mn;
      if (t > n) { n = t; used = 0; }
      if (w >= 0) while (n < GN && used >= 2 - g_rd(n) - g_wt(n) + (n < 32 && (n & 3) >= GXH)) { ++n; used = 0; }
      S.g[S.n] = g; S.k[S.n] = k; S.pos[S.n++] = n;
      used = w >= 0 ? used + w : -g_rd(n);
    }
  };
  add(0, 0, 6, 1); add(0, 6, 10, 0, 1, 1, 1); add(0, 10, 11, 0, 1, 2); add(0, 11, 12, 0, 1); add(0, 12, 19);
  add(1, 0, 8, 0, GL1);
  add(2, 0, 4, 32); add(2, 4, 8, 35); add(2, 8, 54);
  add(3, 0, 2, 79); add(3, 2, 4, 80); add(3, 4, 5, 0, 0, -1); add(3, 5, 7); add(3, 7, 8, 0, 0, -1); add(3, 8, 12);
  add(4, 0, 9); add(4, 9, 11, 0, 1); add(5, 0, 1); add(4, 11, 12, 0, 1); add(5, 1, 2); add(4, 12, 14, 0, 1); add(5, 2, 3); add(4, 14, 15, 0, 1); add(5, 3, 4);
  add(4, 15, 16, 0, 1); add(4, 16, 17, 0, 1); add(4, 17, 21, 0, 1);
  add(4, 21, 25, 0, 0, 2); add(5, 4, 8); add(5, 15, 16); add(4, 25, 31); add(5, 8, 10); add(4, 31, 33, 0, 0, -1); add(5, 10, 15); add(4, 33, 34, 0, 1, 2); add(4, 34, 38, 0, 1); add(4, 38, 40, 0, 1);
  for (int i = 0, c = 0; i <= GN + 1; ++i) { while (c < S.n && S.pos[c] < i) ++c; S.first[i] = c; }
  return S;
}
constexpr GSched GSC = g_sched();
constexpr int g_at(int g, int k) { for (int i = 0; i < GSC.n; ++i) if (GSC.g[i] == g && GSC.k[i] == k) return GSC.pos[i]; return -1; }
static_assert(g_at(1, 3) < 31 && g_at(1, 7) < 32 && g_at(0, 18) < 26 && g_at(2, 53) < 79 && g_at(3, 5) >= 81 && g_at(4, 0) >= g_at(3, 7) + 2 && g_at(5, 3) + 5 < 32 + 3 * (GW - 16)
              && g_at(4, 33) >= g_at(4, 32) + 2 && g_at(4, 39) < GN, "schedule");
template <int LY>
__device__ __forceinline__ void g_att(char* smem, rsrc_t wrs, int oo, int g, const GPre& P, bf16x8 (&wf)[48]) {
  u32 ap = P.ap;
  const LAS char *fr = (const LAS char*)(size_t)P.fr, *fq = (const LAS char*)(size_t)P.fq;
  LAS float* fw = (LAS float*)(size_t)P.fw;
  constexpr int cso = 512, D = GPF;
  const LAS f32x4* sp = (const LAS f32x4*)(size_t)P.sp + 96;
  const u32 sl = P.sl;
  u32 su;
  u64 PAR = 0xAAAAAAAAAAAAAAAAull, VM[4] = {0x0fff00ff000f0000ull, 0x3fff03ff003f0003ull, 0xf000ff00fff0ffffull, 0xc000fc00ffc0fffcull};
  u32 cn = 0xff800000u, co = 0x3F803F80u, cu = 0x03020302u;
  int wo_ = L_WX + g * 3072 - 2 * L_TB;
  PAGEV(10 + LY);
  lds_barrier();
  ST(LY, 13);
  bf16x4 xl[48], xh[48];
  bf16x8 wk, wv, wq;
  f32x4 aK[3], aV[3], aQ[2], s1, s2, cK, cQ, m4, rv, S0, S1, oa;
  f32x2 rk[3], rq, mr;
  float t0, t1, t2, t3, mh, cV, tk[4], tv[4], b[4], l[4];
  u32 u0, wp, pw[2], ow[4], ao;
  u32x2 fk[3], fq0;
  u32x4 fv, pp, on;
#define GTO(s_) ((s_) < 16 ? L_TH - L_TB : (s_) < 32 || !LY ? 0 : 16384)
#define GKS(s_) ((s_) < 16 ? (s_) : (s_) < 32 ? ((s_) - 1) & 15 : (s_) == GW ? 15 : (s_) - 32 - ((s_) > GW))
#define LDL(s_) xl[s_] = __builtin_amdgcn_ds_read_tr16_b64_v4i16((LAS bf16x4*)((const LAS char*)(size_t)ap + GTO(s_) + GKS(s_) * 1024))
#define LDH(s_) xh[s_] = __builtin_amdgcn_ds_read_tr16_b64_v4i16((LAS bf16x4*)((const LAS char*)(size_t)ap + GTO(s_) + GKS(s_) * 1024 + 512))
#define WPTR() wp = vlsa_(ap, wo_)
#define LDW(e_) (*(const LAS bf16x8*)((const LAS char*)(size_t)wp + (e_) * 1024))
#pragma unroll
  for (int s = 0; s < 2; ++s) { LDL(s); LDH(s); }
  s1 = sp[0]; s2 = sp[48];
  SB();
#pragma unroll
  for (int s = 2; s < D; ++s) { LDL(s); LDH(s); }
  SB();
  SPIN(PAR); SPIN(VM[0]); SPIN(VM[1]); SPIN(VM[2]); SPIN(VM[3]); SPIN(cn); SPIN(co); SPIN(cu); SPIN(wo_); SPIN(oo);
  const float eps = vmov_(0x3727c5acu);
  auto op = [&](auto I_) __attribute__((always_inline)) {
    constexpr int p = GSC.g[decltype(I_)::value], o = GSC.k[decltype(I_)::value];
    if constexpr (p == 0) {
      if constexpr (o == 0) t0 = vadd_(s1[0], s1[1]);
      if constexpr (o == 1) t1 = vadd_(s1[2], s1[3]);
      if constexpr (o == 2) t2 = vadd_(s2[0], s2[1]);
      if constexpr (o == 3) t3 = vadd_(s2[2], s2[3]);
      if constexpr (o == 4) t0 = vadd_(t0, t1);
      if constexpr (o == 5) t2 = vadd_(t2, t3);
      if constexpr (o == 6) { t0 *= (1.f / 512.f); PIN(t0); }
      if constexpr (o == 7) t1 = vfman_(eps, t0, t0);
      if constexpr (o == 8) { t1 = fmaf(t2, 1.f / 512.f, t1); PIN(t1); }
      if constexpr (o == 9) t1 = vmax_(t1, eps);
      if constexpr (o == 10) { t1 = __builtin_amdgcn_rsqf(t1); PIN(t1); }
      if constexpr (o == 11) { fw[0] = t1; fw[64] = t0; asm volatile("" ::: "memory"); }
      if constexpr (o == 12) mr = LD2(fr, 256, 64);
      if constexpr (o == 13) cQ = LD4(fq, cso);
      if constexpr (o == 14) cK = LD4(fq, cso + 64);
      if constexpr (o == 15) WPTR();
      if constexpr (o == 16) wk = LDW(1);
      if constexpr (o == 17) wv = LDW(2);
      if constexpr (o == 18) wq = LDW(0);
    }
    if constexpr (p == 1) {
      if constexpr (IN(o, 0, 4)) aQ[0][o] = vmuln_(mr[0], cQ[o]);
      if constexpr (IN(o, 4, 4)) aK[0][o - 4] = vmuln_(mr[0], cK[o - 4]);
    }
    if constexpr (p == 2) {
      if constexpr (o == 0) mh = LD1(fr, 256 + 128);
      if constexpr (o == 1) cV = LD1(fr, cso + 128);
      if constexpr (o == 2) m4 = LD4(fq, 256 + 128);
      if constexpr (o == 3) rv = LD4(fq, 128);
      if constexpr (IN(o, 4, 4)) aQ[1][o - 4] = vmuln_(mr[1], cQ[o - 4]);
      if constexpr (IN(o, 8, 4)) tk[o - 8] = vfman_(aK[2][o - 8], mh, cK[o - 8]);
      if constexpr (IN(o, 12, 4)) aV[2][o - 12] = vfman_(aV[2][o - 12], m4[o - 12], cV);
      if constexpr (o == 16) m4 = LD4(fq, 256 + 64);
      if constexpr (IN(o, 17, 2)) fk[2][o - 17] = vcvt_(tk[2 * (o - 17)], tk[2 * (o - 17) + 1]);
      if constexpr (IN(o, 19, 4)) tk[o - 19] = vmul_(aV[2][o - 19], rv[o - 19]);
      if constexpr (o == 23) rv = LD4(fq, 0);
      if constexpr (IN(o, 24, 2)) fv[o - 24] = vcvt_(tk[2 * (o - 24)], tk[2 * (o - 24) + 1]);
      if constexpr (IN(o, 26, 4)) aV[1][o - 26] = vmuln_(m4[o - 26], cV);
      if constexpr (o == 30) m4 = LD4(fq, 256);
      if constexpr (o == 31) rq = LD2(fr, 0, 64);
      if constexpr (IN(o, 32, 4)) aK[1][o - 32] = vmuln_(mr[1], cK[o - 32]);
      if constexpr (o == 36) asm volatile("v_and_or_b32 %0, %1, 4, %2" : "=v"(u0) : "v"(sl), "v"(fq));
      if constexpr (o == 37) t0 = vmov_(cn);
      if constexpr (o == 38) rk[2] = LD2((const LAS char*)(size_t)u0, 128, 8);
      if constexpr (o == 39) rk[0] = LD2((const LAS char*)(size_t)u0, 0, 8);
      if constexpr (IN(o, 40, 4)) tv[o - 40] = vmuln_(m4[o - 40], cV);
      if constexpr (IN(o, 44, 4)) b[o - 44] = vsel0_(t0, VM[o - 44]);
      if constexpr (IN(o, 48, 4)) tv[o - 48] = vmul_(tv[o - 48], rv[o - 48]);
      if constexpr (IN(o, 52, 2)) { rq[o - 52] *= 0.25f * 1.4426950408889634f; PIN(rq[o - 52]); }
    }
    if constexpr (p == 3) {
      if constexpr (IN(o, 0, 2)) fq0[o] = vcvt_(aQ[0][2 * o], aQ[0][2 * o + 1]);
      if constexpr (IN(o, 2, 2)) fk[0][o - 2] = vcvt_(aK[0][2 * o - 4], aK[0][2 * o - 3]);
      if constexpr (o == 4) mm16z_(S0, fk[2], fq0);
      if constexpr (IN(o, 5, 2)) vfmac_(tv[o - 5], aV[0][o - 5], rv[o - 5]);
      if constexpr (o == 7) mm16z_(S1, fk[0], fq0);
      if constexpr (IN(o, 8, 2)) vfmac_(tv[o - 6], aV[0][o - 6], rv[o - 6]);
      if constexpr (IN(o, 10, 2)) fv[o - 8] = vcvt_(tv[2 * (o - 10)], tv[2 * (o - 10) + 1]);
    }
    if constexpr (p == 4) {
      if constexpr (IN(o, 0, 2)) l[o] = vsel_(S0[2 * o], S0[2 * o + 1], PAR);
      if constexpr (IN(o, 2, 2)) l[o] = vsel_(S1[2 * o - 4], S1[2 * o - 3], PAR);
      if constexpr (IN(o, 4, 4)) l[o - 4] = vfma_(l[o - 4], rk[o < 6 ? 2 : 0][o & 1], b[o - 4]);
      if constexpr (o == 8) t0 = vmax_(l[0], l[1]);
      if constexpr (o == 9) t2 = vmax3_(t0, l[2], l[3]);
      if constexpr (o == 10) t3 = vmax3_(t0, l[2], l[3]);
      if constexpr (o == 11) vsw16_(t2, t3);
      if constexpr (o == 12) t0 = vmax_(t2, t3);
      if constexpr (o == 13) t1 = vmax_(t2, t3);
      if constexpr (o == 14) vsw32_(t0, t1);
      if constexpr (o == 15) t0 = vmax_(t0, t1);
      if constexpr (o == 16) t0 = vmuln_(t0, rq[0]);
      if constexpr (IN(o, 17, 4)) l[o - 17] = vfma_(l[o - 17], rq[0], t0);
      if constexpr (IN(o, 21, 4)) l[o - 21] = vexp_(l[o - 21]);
      if constexpr (IN(o, 25, 2)) pw[o - 25] = vcvt_(l[2 * (o - 25)], l[2 * (o - 25) + 1]);
      if constexpr (IN(o, 27, 4)) pp[o - 27] = vperm_(pw[(o - 27) >> 1], o & 1 ? sl : su);
      if constexpr (o == 31) mmz_(S0, on, pp);
      if constexpr (o == 32) mmz_(oa, fv, pp);
      if constexpr (o == 33) { t0 = vrcp_(S0[0]); asm volatile("" :: "v"(S0)); }
      if constexpr (IN(o, 34, 4)) tk[o - 34] = vmul_(oa[o - 34], t0);
      if constexpr (IN(o, 38, 2)) ow[o - 38] = vcvt_(tk[2 * (o - 38)], tk[2 * (o - 38) + 1]);
    }
    if constexpr (p == 5) {
      static_assert(L_TB % 512 == 0, "lane bits of ap");
      if constexpr (o == 0 && !LY) { asm volatile("v_bitop3_b32 %0, %1, 32, %2 bitop3:0x28" : "=v"(wp) : "v"(ap), "s"(0x1FF)); wp = vlsa_(wp, wo_ + 2 * L_TB); }
      if constexpr (o == 0 && LY) WPTR();
      if constexpr (o == 1) wq = LDW(0);
      if constexpr (o == 2) wk = LDW(1);
      if constexpr (o == 3) wv = LDW(2);
      if constexpr (IN(o, 4, 4)) on[o - 4] = __float_as_uint(vmov_(co));
      if constexpr (o == 8) rk[1] = LD2((const LAS char*)(size_t)u0, 64, 8);
      if constexpr (o == 9) rv = LD4(fq, 64);
      if constexpr (o == 10 && !LY) { static_assert(g_at(5, 10) > 32 + 3 * (47 - GPF - 16) + 1, "last read"); ap ^= 32u; PIN(ap); }
      if constexpr (o == 10) asm volatile("v_and_b32 %0, 0x80, %1" : "=v"(ao) : "v"(ap));
      if constexpr (o == 11) asm volatile("v_and_b32 %0, 0x78, %1" : "=v"(u0) : "v"(ap));
      if constexpr (o == 12) asm volatile("v_and_b32 %0, 0x178, %1" : "=v"(wp) : "v"(ap));
      if constexpr (o == 13) asm volatile("v_lshl_add_u32 %0, %0, 3, %1" : "+v"(ao) : "s"(oo));
      if constexpr (o == 15) asm volatile("v_pk_max_i16 %0, %1, %2" : "=v"(su) : "v"(sl), "s"(cu));
      if constexpr (o == 14) asm volatile("v_add3_u32 %0, %0, %1, %2" : "+v"(ao) : "v"(u0), "v"(wp));
    }
  };
  auto slot = [&](auto N_) __attribute__((always_inline)) {
    constexpr int n = decltype(N_)::value, s = g_step(n), j = g_pos(n), ks = GKS(s);
    constexpr int m = n < 32 ? j + 1 : j;
    constexpr int t = n < 32 ? 2 : n < 80 ? 0 : 1;
    SB();
    if constexpr (j == 0 && !(s & 1)) touch_(xl[s + 1], xh[s + 1]);
    {
      const bf16x8 x = __builtin_shufflevector(xl[s], xh[s], 0, 1, 2, 3, 4, 5, 6, 7);
      if constexpr (ks == 15 && m == 1) mma_(aK[t], wk, x);
      else if constexpr (n == 34) mmz_(aV[t], __builtin_bit_cast(u32x4, x), __builtin_bit_cast(u32x4, wv));
      else if constexpr (ks == 15 && m == 2) mma_(aV[t], x, wv);
      else if constexpr (ks == 15 && m == 0) mma_(aQ[t & 1], wq, x);
      else mma_w<3 * ks + m, n < 2>(m == 1 ? aK[t] : m == 2 ? aV[t] : aQ[t & 1], wf[3 * ks + m], x);
    }
    SB();
    if constexpr (j == 0 && s + D < 48) LDL(s + D);
    if constexpr (j == 1 && s + D < 48) LDH(s + D);
    SB();
    static_for<GSC.first[n], GSC.first[n + 1]>(op);
    static_assert(g_at(0, 15) < 22, "");
    if constexpr (!LY && n == 23) { u32& a_ = ap; a_ += (a_ & 32u) << 9; PIN(a_); }
    if constexpr (!LY && n == 32 + 3 * (32 - GPF - 16) - 1) { u32& a_ = ap; a_ ^= 32u; PIN(a_); }
    if constexpr (n == GN - 2) { f32x4& r_ = rv; PIN(r_); }
  };
  static_for<0, GN>(slot);
  SB();
  ST(LY, 14);
  asm volatile(
      "v_cvt_pk_bf16_f32 v8, v8, v9\n\t"
      "v_cvt_pk_bf16_f32 v9, v10, v11\n\t"
      "s_nop 1\n\t"
      "v_mfma_f32_16x16x16_bf16 v[20:23], %[fk0], v[8:9], 0\n\t"
      "v_cvt_pk_bf16_f32 v4, v4, v5\n\t"
      "v_cvt_pk_bf16_f32 v5, v6, v7\n\t"
      "v_mul_f32 v12, v12, v16\n\t"
      "v_mul_f32 v13, v13, v17\n\t"
      "v_mfma_f32_16x16x16_bf16 v[24:27], v[4:5], v[8:9], 0\n\t"
      "v_mul_f32 v14, v14, v18\n\t"
      "v_mul_f32 v15, v15, v19\n\t"
      "v_cvt_pk_bf16_f32 v32, v12, v13\n\t"
      "v_cndmask_b32_e64 v20, v20, v21, %[par]\n\t"
      "v_cndmask_b32_e64 v21, v22, v23, %[par]\n\t"
      "v_fma_f32 v20, v20, %[r00], %[b0]\n\t"
      "v_fma_f32 v21, v21, %[r01], %[b1]\n\t"
      "v_max_f32 v10, v20, v21\n\t"
      "v_cndmask_b32_e64 v22, v24, v25, %[par]\n\t"
      "v_cndmask_b32_e64 v23, v26, v27, %[par]\n\t"
      "v_fma_f32 v22, v22, %[r10], %[b2]\n\t"
      "v_fma_f32 v23, v23, %[r11], %[b3]\n\t"
      "v_max3_f32 v25, v10, v22, v23\n\t"
      "v_max3_f32 v24, v10, v22, v23\n\t"
      "v_cvt_pk_bf16_f32 v33, v14, v15\n\t"
      "s_nop 0\n\t"
      "v_permlane16_swap_b32 v24, v25\n\t"
      "v_max_f32 v26, v24, v25\n\t"
      "v_max_f32 v27, v24, v25\n\t"
      "s_nop 1\n\t"
      "v_permlane32_swap_b32 v26, v27\n\t"
      "v_max_f32 v24, v26, v27\n\t"
      "v_mul_f32_e64 v24, -v24, %[sc]\n\t"
      "v_fma_f32 v20, v20, %[sc], v24\n\t"
      "v_fma_f32 v21, v21, %[sc], v24\n\t"
      "v_exp_f32 v20, v20\n\t"
      "v_exp_f32 v21, v21\n\t"
      "v_fma_f32 v22, v22, %[sc], v24\n\t"
      "v_fma_f32 v23, v23, %[sc], v24\n\t"
      "v_exp_f32 v22, v22\n\t"
      "v_exp_f32 v23, v23\n\t"
      "v_cvt_pk_bf16_f32 v24, v20, v21\n\t"
      "v_cvt_pk_bf16_f32 v26, v22, v23\n\t"
      "v_perm_b32 v6, v24, v24, %[sl]\n\t"
      "v_perm_b32 v7, v24, v24, %[su]\n\t"
      "v_perm_b32 v4, v26, v26, %[sl]\n\t"
      "v_perm_b32 v5, v26, v26, %[su]\n\t"
      "s_nop 1\n\t"
      "v_mfma_f32_16x16x32_bf16 v[20:23], %[on], v[4:7], 0\n\t"
      "v_mfma_f32_16x16x32_bf16 v[8:11], v[32:35], v[4:7], 0\n\t"
      "s_nop 3\n\t"
      "v_rcp_f32 v24, v20\n\t"
      "s_nop 2\n\t"
      "v_mul_f32 v8, v8, v24\n\t"
      "v_mul_f32 v9, v9, v24\n\t"
      "v_mul_f32 v10, v10, v24\n\t"
      "v_mul_f32 v11, v11, v24\n\t"
      "v_cvt_pk_bf16_f32 v30, v8, v9\n\t"
      "v_cvt_pk_bf16_f32 v31, v10, v11\n\t"
      "s_nop 0\n\t"
      "v_permlane16_swap_b32 v28, v30\n\t"
      "v_permlane16_swap_b32 v29, v31\n\t"
      : "+{v[4:7]}"(aK[1]), "+{v[8:11]}"(aQ[1]), "+{v[12:15]}"(aV[1]), "=&{v[20:23]}"(S0), "=&{v[24:27]}"(S1), "+{v28}"(ow[0]), "+{v29}"(ow[1]), "=&{v30}"(ow[2]), "=&{v31}"(ow[3]), "+{v[32:35]}"(fv)
      : "{v[16:19]}"(rv), [sc] "v"(rq[1]), [par] "s"(PAR), [fk0] "v"(fk[0]), [on] "v"(on), [r00] "v"(rk[0][0]), [r01] "v"(rk[0][1]), [r10] "v"(rk[1][0]), [r11] "v"(rk[1][1]),
        [b0] "v"(b[0]), [b1] "v"(b[1]), [b2] "v"(b[2]), [b3] "v"(b[3]), [sl] "v"(sl), [su] "v"(su));
  u32x4 ov = {ow[0], ow[1], ow[2], ow[3]};
  stw(wrs, (int)ao, ov);
  asm volatile("s_nop 1" :: "v"(ov) : "memory");
}
template <bool L1>
__device__ __forceinline__ void g_blayer(char* smem, rsrc_t wrs, int g, u32 tag, GPre& P, GSum& S, bf16x8 (&wf)[48]) {
  const int bid = BIDX;
  const int rb = (bid & 7) * 4 + (bid >> 6), hb = (bid >> 3) & 7;
  const int mo = KVO(rb, !L1, 0, hb * 4 + g);
  if (!L1) ST(1, 10);
  int oo = (L1 ? OFF_O1 : OFF_O2) + MB_ + ((rb * 32 + (hb * 2 + (g >> 1)) * 2) * 64 + 32 * (g & 1)) * 16;
  g_gemm<!L1, 0>(smem, wrs, mo, 0, 0, tag, g, P, S, wf);
  ST(!L1, 12);
  if constexpr (!(L1 ? EX1 : EX2)) g_att<!L1>(smem, wrs, oo, g, P, wf);
  else g_kv<!L1>(smem, wrs, KVO(rb, !L1, 1, hb * 4 + g), KVO((rb - 1) & 31, !L1, rb != 0, hb * 4 + g), oo, tag, g, P, wf);
  ST(!L1, 16);
  if (L1) PAGE(12);
  __builtin_amdgcn_s_sleep(L1 ? SLP_OB1 : SLP_OB2);
  cnt_add(smem, C_OB, 0);
}

using u32x8 = __attribute__((ext_vector_type(8))) unsigned;
using u32x16 = __attribute__((ext_vector_type(16))) unsigned;
__global__ __launch_bounds__(512, 2) KATTR void k_mega(const u16*, const u16*, const u16*, const u16*, const u16*, const u16*, const u16*, const u16*,
                                                       const u16*, const u16*, const u16*, char*, u32*) {
  extern __shared__ __attribute__((aligned(16))) char smem[];
  u32x16 ka;
  u32x8 kb;
  u32x2 kc;
  asm volatile("s_load_dwordx16 %0, %3, 0x0\n\ts_load_dwordx8 %1, %3, 0x40\n\ts_load_dwordx2 %2, %3, 0x60"
               : "=&s"(ka), "=&s"(kb), "=&s"(kc) : "s"(__builtin_amdgcn_kernarg_segment_ptr()) : "memory");
  const int tid = TIDX, lane = tid & 63;
  const int w = __builtin_amdgcn_readfirstlane(tid >> 6);
  const int bid = BIDX;
  ST_INIT(); ST(0, 0); ST(1, 0);
  if (w == 0) {
    u64 pc;
    int clen;
    asm volatile("s_getpc_b64 %0\n.Lcw_base:\n\ts_mov_b32 %1, .Lcw_end-.Lcw_base-4" : "=s"(pc), "=s"(clen));
    asm volatile("s_mov_b32 m0, %2\n\ts_nop 0\n\tglobal_load_lds_dword %0, %1" :: "v"(min(min(lane, 15) * 128 + (bid >> 3) * 2048, clen)), "s"(pc), "s"(L_FS) : "memory");
  }
  const int kp = (bid & 7) * 32 + (bid >> 3), hb = (bid >> 3) & 7;
  const int c = (w >> 1) & 1, g = w & 3;
  u32 po = (u32)(kp * 512 + (tid & 255) * 2);
  u32 xo = (u32)((w < 4 ? ((bid >> 3) * 64 + 16 * w) * 1024 + (bid & 7) * 128 : bid * 8192 + g * 2048) + (lane & 7) * 16 + (lane >> 3) * (w < 4 ? 2048 : 128));
  asm volatile("" : "+v"(po), "+v"(xo) :: "memory");
  asm volatile("s_waitcnt lgkmcnt(0)" : "+s"(ka), "+s"(kb), "+s"(kc) :: "memory");
#define KP(T_, v_, i_) ((T_)(((u64)v_[2 * (i_) + 1] << 32) | v_[2 * (i_)]))
  const u16 *x0 = KP(const u16*, ka, 0), *A_re = KP(const u16*, ka, 1), *A_im = KP(const u16*, ka, 2), *B_re = KP(const u16*, ka, 3), *B_im = KP(const u16*, ka, 4);
  const u16 *C_re = KP(const u16*, ka, 5), *C_im = KP(const u16*, ka, 6), *log_dt = KP(const u16*, ka, 7), *Dskip = KP(const u16*, kb, 0), *wqkv = KP(const u16*, kb, 1), *wo = KP(const u16*, kb, 2);
  char* ws = KP(char*, kb, 3);
  u32* out = KP(u32*, kc, 0);
  u32x4 fx[2], fp, fq;
  u32 ldtw, dskw;
#define LD_A "global_load_ushort v248, %[po], %[ar]\n\tglobal_load_ushort v249, %[po], %[ai]\n"
  asm volatile("s_cmp_gt_u32 %[w], 3\n\ts_cbranch_scc1 0f\n\ts_load_dword %[dt], %[pd], %[k4]\n\ts_load_dword %[ds], %[ps], %[k4]\n\t" LD_A
               "0:\n\tglobal_load_dwordx4 v[240:243], %[xo], %[x0]\n\tglobal_load_dwordx4 v[244:247], %[xo], %[x0] offset:1024\n\tglobal_load_dword v254, %[eo], %[ws] sc1\n\t"
               "s_cbranch_scc0 1f\n\t"
               "global_load_ushort v250, %[po], %[br]\n\tglobal_load_ushort v251, %[po], %[bi]\n\tglobal_load_ushort v252, %[po], %[cr]\n\tglobal_load_ushort v253, %[po], %[ci]\n\t" LD_A "1:"
               : "=&{v[240:243]}"(fx[0]), "=&{v[244:247]}"(fx[1]), "=&{v[248:251]}"(fp), "=&{v[252:255]}"(fq), [dt] "=&s"(ldtw), [ds] "=&s"(dskw)
               : [w] "s"(w), [po] "v"(po), [xo] "v"(xo), [eo] "v"(OFF_EP), [k4] "s"(4 * kp), [x0] "s"(x0), [ar] "s"(A_re), [ai] "s"(A_im), [br] "s"(B_re), [bi] "s"(B_im),
                 [cr] "s"(C_re), [ci] "s"(C_im), [pd] "s"(log_dt), [ps] "s"(Dskip), [ws] "s"(ws) : "memory", "scc");
  ST(2, 1);
  __builtin_amdgcn_sched_barrier(0);
  const rsrc_t wrs = __builtin_amdgcn_make_buffer_rsrc((void*)ws, 0, WSB, 0x00020000);
  if (w >= 4) {
    asm volatile("s_mov_b32 m0, %2\n\ts_nop 0\n\tglobal_load_lds_dword %0, %1" :: "v"((bid * 33 + min(g * 9 + min(lane, 8), 32)) * 128), "s"(wqkv), "s"(L_FS + w * 256) : "memory");
    u32x4 zz = {0, 0, 0, 0};
    asm volatile("" : "+v"(zz));
    const int z0 = (int)opaque((u32)(L_U + (lane >> 5) * 4608 + (lane & 31) * 16)), z1 = (int)opaque((u32)(L_MI + min((tid & 255) * 16 - 1024, 1088)));
    *(u32x4*)(smem + (g == 0 ? z0 : z1)) = zz;
    asm volatile("s_nop 1\n\ts_waitcnt lgkmcnt(0)" :: "v"(zz) : "memory");
  }
  __builtin_amdgcn_s_barrier();
  __builtin_amdgcn_sched_barrier(0);
  const int ftb = c ? L_TA + 6400 : 0;
#define PUT(e_, r_, i_) *(LAS u32*)(wa + (e_) * FTS) = pkh(r_, i_)
#define CMR(a_, b_) fmaf(a_[0], b_[0], -(a_[1] * b_[1]))
#define CMI(a_, b_) fmaf(a_[0], b_[1], a_[1] * b_[0])
#define CMUL(a_, b_) (f32x2){CMR(a_, b_), CMI(a_, b_)}
  struct P0A { u32 gad[12], sad[8], ua, ur; };
  const u32 sb = (u32)((L_KT + c * TBS - 0x7E00) & 0xFFFF) * 0x10001u;
  auto p0_gad = [&](P0A& A, int lane) __attribute__((always_inline)) {
    const float f0 = __uint_as_float(opaque(__float_as_uint((float)(1023 - 512 * (w & 1) - 8 * lane))));
#pragma unroll
    for (int e = 0; e < 6; ++e) { const u32 ga = tb_addr(pk2(f0 - (float)(2 * e), f0 - (float)(2 * e + 1)), sb); A.gad[2 * e] = ga & 0xFFFFu; A.gad[2 * e + 1] = ga >> 16; }
    asm volatile("" : "+v"(A.gad[0]), "+v"(A.gad[1]), "+v"(A.gad[2]), "+v"(A.gad[3]), "+v"(A.gad[4]), "+v"(A.gad[5]), "+v"(A.gad[6]), "+v"(A.gad[7]), "+v"(A.gad[8]), "+v"(A.gad[9]), "+v"(A.gad[10]));
    __builtin_amdgcn_sched_barrier(0);
  };
  auto p0_sad = [&](P0A& A, int lane) __attribute__((always_inline)) {
    lane = (int)opaque((u32)lane);
    const int i = lane & 15, kk = lane >> 4;
    const int e128 = L_KT + c * TBS + 2 * (0x4300 - 0x3F00);
    if (w & 1) {
      const int sh = i >> 3, base = e128 + (i < 8 ? 256 + (i >> 2) * 128 + (i & 3) * 2 : 256 + (i >> 1) * 64 + (i & 1) * 2) + (kk << (5 - sh));
#pragma unroll
      for (int e = 0; e < 4; ++e) A.sad[e] = base + (e << (3 - sh));
    } else {
      const float fb = (float)(128 * (i >> 3) + (i & 7) + 32 * kk);
      const u32 s0 = tb_addr(pk2(fb, fb + 8.f), sb), s1 = tb_addr(pk2(fb + 16.f, fb + 24.f), sb);
      A.sad[0] = s0 & 0xFFFFu; A.sad[1] = s0 >> 16; A.sad[2] = s1 & 0xFFFFu; A.sad[3] = s1 >> 16;
    }
    A.sad[4] = e128 + 768 + min(32 * i, 258) + 8 * kk;
    asm volatile("" : "+v"(A.sad[0]), "+v"(A.sad[1]), "+v"(A.sad[2]), "+v"(A.sad[3]), "+v"(A.sad[4]));
    __builtin_amdgcn_sched_barrier(0);
  };
  auto k_unit = [&](P0A& A, float dk, bool lag0) __attribute__((always_inline)) {
    f16x8 fr[8], fc[8];
    const f32x2 bd = *(const f32x2*)(smem + L_P0 + 512 + c * 8);
    __builtin_amdgcn_sched_barrier(0);
#pragma unroll
    for (int e = 0; e < 8; ++e) {
      fc[e] = *(const LAS f16x8*)(size_t)(A.ua + e * 64 + 41 * FTS);
      fr[e] = *(const LAS f16x8*)(size_t)(A.ur + e * 64);
      __builtin_amdgcn_sched_barrier(0);
    }
    f16x8 f2[8];
    p0_sad(A, lane);
    const int ec = bexp(bd[0] + bd[1] + fabsf(dk));
    float f0 = __uint_as_float((u32)max(127 + bexp(bd[0]) - ec, 0) << 23), f1 = __uint_as_float((u32)max(127 + bexp(bd[1]) - ec, 0) << 23);
    *(float*)(smem + L_MI + 4 * c) = __uint_as_float((u32)(ec - 14) << 23);
    float d0s = lag0 ? dk * __uint_as_float((u32)(268 - ec) << 23) : 0.f;
    asm volatile("" : "+v"(f0), "+v"(f1), "+v"(d0s));
    auto unit = [&](const f16x8 (&fr)[8], u32x4 sa, float add, bool more) __attribute__((always_inline)) {
      f32x4 k0, k1;
#pragma unroll
      for (int e = 0; e < 8; ++e) {
        if (e & 3) asm volatile("v_mfma_f32_16x16x32_f16 %0, %1, %2, %0" : "+v"(e < 4 ? k0 : k1) : "v"(fc[e]), "v"(fr[e]));
        else asm volatile("v_mfma_f32_16x16x32_f16 %0, %1, %2, 0" : "=&v"(e < 4 ? k0 : k1) : "v"(fc[e]), "v"(fr[e]));
        __builtin_amdgcn_sched_barrier(0);
        if (more) f2[e] = *(const LAS f16x8*)(size_t)(A.ua + e * 64 + 32 * FTS);
        __builtin_amdgcn_sched_barrier(0);
      }
      asm volatile("" : "+v"(k0));
      k0[0] = fmaf(k0[0], f0, add); k0[1] *= f0; k0[2] *= f0; k0[3] *= f0;
      asm volatile("s_nop 3" : "+v"(k0), "+v"(k1));
      const u32 h01 = pkh(fmaf(k1[0], f1, k0[0]), fmaf(k1[1], f1, k0[1])), h23 = pkh(fmaf(k1[2], f1, k0[2]), fmaf(k1[3], f1, k0[3]));
      *(LAS u16*)((LAS char*)(size_t)sa[0]) = (u16)h01; *(LAS u16*)((LAS char*)(size_t)sa[1]) = (u16)(h01 >> 16);
      *(LAS u16*)((LAS char*)(size_t)sa[2]) = (u16)h23; *(LAS u16*)((LAS char*)(size_t)sa[3]) = (u16)(h23 >> 16);
    };
    if (!(w & 1)) {
      unit(fr, (u32x4){A.sad[0], A.sad[1], A.sad[2], A.sad[3]}, d0s, true);
      cnt_add(smem, C_A, lane);
      unit(f2, (u32x4){A.sad[4], A.sad[4] + 2, A.sad[4] + 4, A.sad[4] + 6}, 0.f, false);
    } else {
      unit(fr, (u32x4){A.sad[0], A.sad[1], A.sad[2], A.sad[3]}, 0.f, false);
      cnt_add(smem, C_A, lane);
    }
  };
  auto k_copies = [&](const P0A& A, u32 cop, u32 keep) __attribute__((always_inline)) {
    u32 kh[11], kw[6], sh[5];
#pragma unroll
    for (int e = 0; e < 11; ++e) kh[e] = *(const LAS u16*)(size_t)A.gad[e];
    __builtin_amdgcn_sched_barrier(0);
#pragma unroll
    for (int e = 0; e < 5; ++e) kw[e] = __builtin_amdgcn_perm(kh[2 * e + 1], kh[2 * e], e == 4 ? keep : 0x05040100u);
    kw[5] = __builtin_amdgcn_perm(kh[10], kh[10], keep);
#pragma unroll
    for (int k = 0; k < 5; ++k) sh[k] = __builtin_amdgcn_alignbit(kw[k + 1], kw[k], 16);
    put_copies(cop, (u32x4){kw[0], kw[1], kw[2], kw[3]}, (u32x4){sh[0], sh[1], sh[2], sh[3]}, (u32x4){kw[1], kw[2], kw[3], kw[4]}, (u32x4){sh[1], sh[2], sh[3], sh[4]});
  };
  __builtin_amdgcn_sched_barrier(0);
  if (w < 4) {
#undef DROLE_
#define DROLE_ 0
  P0A pa;
  C1Pre c1;
  LAS char* wa = (LAS char*)(size_t)opaque((u32)(ftb + (tid & 127) * 4));
  u32 tg;
  const bool lo = bid < 128;
  const int xts = lo ? XBS : XTS, xt2 = lo ? 128 : 2 * XTS;
  const int xto = (int)opaque((u32)(lo ? OFF_X1 + ((bid >> 3) * 8 + 2 * w) * XBS + ((bid & 7) * 32 + 4 * (lane >> 3)) * 64 + (lane & 7) * 16
                                       : XTO((bid & 7) * 32 + 4 * (lane >> 3)) + (((bid >> 3) & 15) * 64 + 16 * w + 2 * (lane & 7)) * 8));
  int xta[4];
  auto xad = [&]() __attribute__((always_inline)) {
#pragma unroll
    for (int e = 0; e < 4; ++e) xta[e] = (int)opaque((u32)(xto + (e & 1) * xts + (e >> 1) * xt2));
  };
  xad();
  const int sl = (int)opaque((u32)(((lane & 7) * 8 + (lane >> 3)) * 4));
  auto xpub = [&]() __attribute__((always_inline)) {
    __builtin_amdgcn_sched_barrier(0);
    asm volatile("s_cmpk_gt_i32 %3, 0x7f\n\ts_waitcnt vmcnt(0)\n\ts_cbranch_scc1 0f\n\t"
                 "v_permlane32_swap_b32 v240, v241\n\tv_permlane32_swap_b32 v244, v245\n\tv_permlane32_swap_b32 v242, v243\n\tv_permlane32_swap_b32 v246, v247\n0:"
                 : "+{v[240:243]}"(fx[0]), "+{v[244:247]}"(fx[1]), "+{v[252:255]}"(fq) : "s"(bid) : "scc", "memory");
    u32x4 q[4];
#pragma unroll
    for (int e = 0; e < 8; ++e) q[e >> 1][2 * (e & 1)] = (u32)__builtin_amdgcn_ds_bpermute(sl, (int)fx[e & 1][e >> 1]);
    __builtin_amdgcn_sched_barrier(0);
    tg = fq[2] + 1;
    const u32 mk = x0_mark(tg);
    u32 m8[8];
#pragma unroll
    for (int e = 0; e < 8; ++e) { m8[e] = mk; asm volatile("" : "+v"(m8[e])); q[e >> 1][2 * (e & 1) + 1] = m8[e]; }
    __builtin_amdgcn_sched_barrier(0);
#pragma unroll
    for (int e = 0; e < 4; ++e) stw(wrs, xta[e], q[e]);
    asm volatile("s_nop 1" :: "v"(q[0]), "v"(q[1]), "v"(q[2]), "v"(q[3]) : "memory");
    ST(0, 3);
    __builtin_amdgcn_sched_barrier(0);
  };
  {
    float ar, ai;
    asm volatile("s_waitcnt vmcnt(3)\n\tv_lshlrev_b32 %0, 16, v248\n\tv_lshlrev_b32 %1, 16, v249" : "=&v"(ar), "=&v"(ai) : "{v[248:251]}"(fp) : "memory");
    asm volatile("s_waitcnt lgkmcnt(0)" : "+s"(ldtw), "+s"(dskw) :: "memory");
    ST(0, 1);
    const float dt = rbf(__expf(c ? hi2f(ldtw) : lo2f(ldtw)));
    const float zr = dt * ar * 1.4426950408889634f, zi = dt * ai * 0.15915494309189535f;
    float cs = __builtin_amdgcn_exp2f(fmaxf(zr, 0.f) * -1928.f);
    f32x2 P[8], Q1, R[17];
    { float a_, b_; cexp2(zr, zi, a_, b_); P[1] = (f32x2){a_, b_}; cexp2(zr * 128.f, zi * 128.f, a_, b_); Q1 = (f32x2){a_, b_}; }
    asm volatile("" : "+v"(cs), "+v"(P[1]), "+v"(Q1));
    P[2] = CMUL(P[1], P[1]);
    const f32x2 Q2 = CMUL(Q1, Q1);
    R[1] = Q1 * cs; R[2] = Q2 * cs;
    P[3] = CMUL(P[2], P[1]); P[4] = CMUL(P[2], P[2]);
    const f32x2 Q4 = CMUL(Q2, Q2);
    R[3] = CMUL(R[1], Q2); R[4] = Q4 * cs;
    P[5] = CMUL(P[4], P[1]); P[6] = CMUL(P[3], P[3]); P[7] = CMUL(P[4], P[3]);
#pragma unroll
    for (int j = 5; j <= 16; ++j) R[j] = CMUL(R[j - 4], Q4);
    asm volatile("" : "+v"(R[13]), "+v"(R[14]), "+v"(R[15]), "+v"(R[16]), "+v"(P[7]), "+v"(P[6]), "+v"(P[5]));
    xpub();
    PUT(0, cs, 0.f); PUT(8, R[1][0], R[1][1]);
#pragma unroll
    for (int r = 1; r < 8; ++r) { PUT(r, P[r][0] * cs, P[r][1] * cs); PUT(8 + r, CMR(R[1], P[r]), CMI(R[1], P[r])); }
    asm volatile("" ::: "memory");
#pragma unroll
    for (int j = 2; j < 4; ++j) {
      PUT(16 + 4 * (j - 2), R[j][0], R[j][1]);
#pragma unroll
      for (int r = 1; r < 4; ++r) PUT(16 + 4 * (j - 2) + r, CMR(R[j], P[2 * r]), CMI(R[j], P[2 * r]));
    }
#pragma unroll
    for (int j = 4; j < 8; ++j) { PUT(24 + 2 * (j - 4), R[j][0], R[j][1]); PUT(25 + 2 * (j - 4), CMR(R[j], P[4]), CMI(R[j], P[4])); }
#pragma unroll
    for (int j = 8; j <= 16; ++j) PUT(24 + j, R[j][0], R[j][1]);
    __builtin_amdgcn_sched_barrier(0);
  }
  ST(0, 2);
  cnt_add(smem, C_F, lane);
  PAGEV(0);
  pa.ua = (u32)(ftb + (lane & 15) * FTS + (lane >> 4) * 16); pa.ur = pa.ua + (w & 1) * 16 * FTS;
  asm volatile("" : "+v"(pa.ua), "+v"(pa.ur));
  p0_gad(pa, lane);
  cnt_spin(smem, C_F, 8);
  k_unit(pa, c ? hi2f(dskw) : lo2f(dskw), (tid & 127) == 0);
  u32x4 zz = {0, 0, 0, 0};
  asm volatile("" : "+v"(zz));
  c1_pre(c1, kp, w, lane);
  const u32 keep = opaque((tid & 127) == 127 ? 0x0c0c0c0cu : 0x05040100u);
  const u32 tl = opaque((u32)(((lane >> 1) & 7) * CSTR + 4096 + (lane & 1) * 16));
  c1.cop = opaque((u32)(c * 4 * CSTR + (tid & 127) * 16));
  cnt_spin(smem, C_A, 4);
  ST(0, 4);
  *(LAS u32x4*)(size_t)tl = zz;
  k_copies(pa, c1.cop + 2048, keep);
  asm volatile("" :: "v"(zz));
  cnt_add(smem, C_A, lane);
  ST(0, 5);
  cnt_spin(smem, C_A, 8);
  bf16x8 wof[16];
  u32x4 pfl, wfl;
  u32 g2, stag = __builtin_amdgcn_readfirstlane(tg);
  asm volatile("" : "+s"(stag));
  CPre cp;
  if (w & 1) a_conv1<1>(smem, wrs, kp, w, lane, c1, pfl, wfl, g2); else a_conv1<0>(smem, wrs, kp, w, lane, c1, pfl, wfl, g2);
  a_blayer<true>(smem, wrs, ws, out, tid, w, wof, stag, pfl, wfl, g2, cp);
  ST(1, 2);
  a_conv(smem, wrs, cp, w, lane);
  a_blayer<false>(smem, wrs, ws, out, tid, w, wof, stag, pfl, wfl, g2, cp);
  ST_OUT();
  asm volatile("s_endpgm\n\t.p2align 12" : "={v[240:243]}"(fx[0]), "={v[244:247]}"(fx[1]), "={v[248:251]}"(fp), "={v[252:255]}"(fq));
  }
  {
#undef DROLE_
#define DROLE_ 1
    asm volatile("s_load_dword %0, %1, %2" : "=s"(ldtw) : "s"(log_dt), "s"(4 * kp) : "memory");
    const int lane = lane_id();
    LAS char* wa = (LAS char*)(size_t)opaque((u32)(ftb + (w & 1) * 256 + lane * 4));
    u32 tag;
    u32 st0, st1;
    auto cols = [&]() __attribute__((always_inline)) {
    {
      float ar, ai, br, bi, cr, ci;
      asm volatile("v_lshlrev_b32 %0, 16, v248\n\tv_lshlrev_b32 %1, 16, v249\n\tv_lshlrev_b32 %2, 16, v250\n\t"
                   "v_lshlrev_b32 %3, 16, v251\n\tv_lshlrev_b32 %4, 16, v252\n\tv_lshlrev_b32 %5, 16, v253"
                   : "=&v"(ar), "=&v"(ai), "=&v"(br), "=&v"(bi), "=&v"(cr), "=&v"(ci) : "{v[248:251]}"(fp), "{v[252:255]}"(fq) : "memory");
      asm volatile("s_waitcnt lgkmcnt(0)" : "+s"(ldtw) :: "memory");
      const float dt = rbf(__expf(c ? hi2f(ldtw) : lo2f(ldtw)));
      const float dbr = dt * br, dbi = dt * bi;
      const float zr = dt * ar * 1.4426950408889634f, zi = dt * ai * 0.15915494309189535f;
      const float zp = fmaxf(zr, 0.f), g1 = __builtin_amdgcn_exp2f(zp * 1928.f);
      const float wr = (cr * dbr - ci * dbi) * g1, wi = (cr * dbi + ci * dbr) * g1;
      float bd = (fabsf(wr) + fabsf(wi)) * __builtin_amdgcn_exp2f(zp * 120.f);
      f32x2 S1, C[16];
      { float a_, b_; cexp2(zr * 8.f, zi * 8.f, a_, b_); S1 = (f32x2){a_, b_}; }
      bd = dpp_add<0x111, 0xf>(bd); bd = dpp_add<0x112, 0xf>(bd);
      const f32x2 S2 = CMUL(S1, S1);
      bd = dpp_add<0x114, 0xf>(bd); bd = dpp_add<0x118, 0xf>(bd);
      const f32x2 S4 = CMUL(S2, S2);
      bd = dpp_add<0x142, 0xa>(bd); bd = dpp_add<0x143, 0xc>(bd);
      bd = __int_as_float(__builtin_amdgcn_readlane(__float_as_int(bd), 63));
      *(float*)(smem + L_P0 + 512 + g * 4) = bd;
      const float sc = __uint_as_float((u32)(268 - bexp(bd)) << 23);
      C[0] = (f32x2){wr * sc, wi * sc};
      C[1] = CMUL(C[0], S1); C[2] = CMUL(C[0], S2); C[3] = CMUL(C[1], S2);
#pragma unroll
      for (int e = 4; e < 16; ++e) C[e] = CMUL(C[e - 4], S4);
#pragma unroll
      for (int e = 0; e < 16; ++e) *(LAS u32*)(wa + (41 + e) * FTS) = pkhn(C[e][0], C[e][1]);
    }
      cnt_add(smem, C_F, lane);
      ST(0, 2);
    };
    auto stats = [&]() __attribute__((always_inline)) {
    {
      const u32 ONES = opaque(0x3F803F80u);
      u32x4 (&xt)[2] = fx;
      float sm[2] = {0.f, 0.f}, sq[2] = {0.f, 0.f};
#pragma unroll
      for (int e = 0; e < 8; ++e) { sm[e & 1] = dot2acc(xt[e & 1][e >> 1], ONES, sm[e & 1]); sq[e & 1] = dot2acc(xt[e & 1][e >> 1], xt[e & 1][e >> 1], sq[e & 1]); }
      const bool b0 = lane & 1, b1 = lane & 2;
      const float v0 = (b0 ? sm[1] : sm[0]) + dpp_mov<DPP_XOR1>(b0 ? sm[0] : sm[1]), v1 = (b0 ? sq[1] : sq[0]) + dpp_mov<DPP_XOR1>(b0 ? sq[0] : sq[1]);
      float v = (b1 ? v1 : v0) + dpp_mov<DPP_XOR2>(b1 ? v0 : v1);
      v += dpp_mov<0x124>(v); v += dpp_mov<DPP_ROR8>(v);
      auto r = __builtin_amdgcn_permlane16_swap(__float_as_uint(v), __float_as_uint(v), false, false);
      v = __uint_as_float(r[0]) + __uint_as_float(r[1]);
      auto q = __builtin_amdgcn_permlane32_swap(__float_as_uint(v), __float_as_uint(v), false, false);
      v = (__uint_as_float(q[0]) + __uint_as_float(q[1])) * (1.f / 512.f);
      st0 = pack_stats(v, fmaxf(dpp_mov<DPP_XOR2>(v) - v * v, 0.f));
      st1 = dpp_u<DPP_XOR1>(st0);
    }
    };
    auto publish = [&]() __attribute__((always_inline)) {
      asm volatile("v_add_u32 %0, 1, v254" : "=v"(tag) : "{v[252:255]}"(fq) : "memory");
      stw(wrs, lane == 0 ? OFF_SG + bid * 64 + g * 16 : WSB, (u32x4){st0, tag, st1, tag});
      __builtin_amdgcn_sched_barrier(0);
    };
#define VMW(n_) asm volatile("s_waitcnt vmcnt(%0)" :: "n"(n_) : "memory")
    __builtin_amdgcn_s_setprio(1);
    VISIT(0);
    asm volatile("s_waitcnt vmcnt(8)" : "+{v[240:243]}"(fx[0]), "+{v[244:247]}"(fx[1]) :: "memory");
    ST(0, 1);
    stats();
    VMW(7);
    publish();
    const bool isq = bid < 128;
    const int us = min(11 + (bid >> 3) / 3, 15), um = (bid >> 3) % 3;
    asm volatile("s_mov_b32 m0, %2\n\ts_nop 0\n\tglobal_load_lds_dwordx4 %0, %1"
                 :: "v"((isq ? (us * 32 + 8 * g) * 3072 + um * 1024 : (((bid >> 3) & 15) * 32 + 8 * g) * 1024) + (bid & 7) * 128 + (lane >> 3) * (isq ? 3072 : 1024) + (((lane & 7) ^ (isq ? (lane >> 3) & 6 : ((lane >> 3) & 2) | ((g & 1) << 2))) << 4)),
                    "s"(isq ? wqkv : wo), "s"(L_WR + g * 1024) : "memory");
    ST(0, 10);
    const rsrc_t qrs = __builtin_amdgcn_make_buffer_rsrc((void*)wqkv, 0, 1572864, 0x00020000);
    const int wvo = (int)opaque((u32)((8 * g + (lane >> 3)) * 3072 + hb * 128 + (((lane & 7) ^ ((lane >> 3) & 6)) << 4)));
#define WSO(s_) ((s_) < 4 ? 40960 + (s_) * 12288 : (s_) == 4 ? L_WX - L_WS : (s_) < 7 ? 8192 + ((s_) - 5) * 12288 : 32768 + ((s_) - 7) * 12288)
    auto wraw = [&](auto S_) __attribute__((always_inline)) { constexpr int s = decltype(S_)::value; w_dma(qrs, wvo, s * 98304, L_WS + WSO(s) + g * 1024); };
    VMW(3);
    ST(0, 20);
    cols();
    __builtin_amdgcn_s_setprio(0);
    static_for<0, R1D>(wraw);
    PAGE(8);
    VISIT(1); VISIT(2);
    VMW(3 * R1D);
    cnt_add(smem, C_B, lane);
    static_for<R1D, R1E>(wraw);
    cnt_spin(smem, C_B, 4);
    {
      const u32 tq = (u32)(L_WR + (lane >> 2) * 128 + (((2 * g + ((lane & 3) >> 1)) ^ ((lane >> 2) & 6)) << 4) + (lane & 1) * 8);
      const u32 to = (u32)(L_WR + (8 * (lane >> 4) + ((lane >> 2) & 3)) * 128 + (((2 * g + ((lane & 3) >> 1)) ^ (((lane >> 2) & 2) | ((lane >> 2) & 4))) << 4) + (lane & 1) * 8);
      const bf16x4 q0 = __builtin_amdgcn_ds_read_tr16_b64_v4i16((LAS bf16x4*)(size_t)(isq ? tq : to)), q1 = __builtin_amdgcn_ds_read_tr16_b64_v4i16((LAS bf16x4*)(size_t)(isq ? tq + 2048 : to + 512));
      stw(wrs, (bid < 120 ? OFF_WQ + us * 98304 + ((bid & 7) * 4 + g) * 3072 + um * 1024 : isq ? WSB : OFF_WO + (((bid & 7) * 16 + ((bid >> 3) & 15)) * 4 + g) * 1024) + lane * 16,
          __builtin_bit_cast(u32x4, __builtin_shufflevector(q0, q1, 0, 1, 2, 3, 4, 5, 6, 7)));
    }
    static_for<R1E, 5>(wraw);
    const int fso = lane == 0 ? OFF_ST0 + 4096 + (bid >> 7) * 4096 + ((bid & 7) * 4 + g) * 128 + ((bid >> 3) & 15) * 4 : WSB;
    ST(0, 15);
    const u32 stag = __builtin_amdgcn_readfirstlane(tag);
    bf16x8 wf[48];
    GPre gp;
    GSum gs_;
    {
      const int ln = lane;
      const int wbo = (int)opaque((u32)((hb * 4 + g) * 3072 + ln * 16));
      const int fo = (int)opaque((u32)(OFF_ST0 + 4096 + (hb * 4 + g) * 128 + (ln & 3) * 16));
      const u32 wga = opaque((u32)(L_WS + (ln >> 2) * 128 + (((2 * g + ((ln & 3) >> 1)) ^ ((ln >> 2) & 6)) << 4) + (ln & 1) * 8)), wgb = opaque(wga + 53248u);
      auto wget = [&](auto S_) __attribute__((always_inline)) {
        constexpr int s = decltype(S_)::value, o = WSO(s) >= 53248 ? WSO(s) - 53248 : WSO(s);
        w_get<3 * s, o>(wf[3 * s], WSO(s) >= 53248 ? wgb : wga);
        w_get<3 * s + 1, o + 4096>(wf[3 * s + 1], WSO(s) >= 53248 ? wgb : wga);
        w_get<3 * s + 2, o + 8192>(wf[3 * s + 2], WSO(s) >= 53248 ? wgb : wga);
      };
      const int r0 = 256 * g + 2 * ln;
      const int sto = (int)opaque((u32)(OFF_SG + r0 * 8)), xto = (int)opaque((u32)(XTO(kp) + r0 * 8));
      const int xlo = (int)opaque((u32)(OFF_X1 + (32 * g + (ln >> 2)) * XBS + kp * 64 + (ln & 3) * 16));
      u32 mk = x0_mark(stag);
      asm volatile("" : "+s"(mk));
#define GXO(e_) ((e_) < 2 ? xlo + (e_) * 16 * XBS : xto + ((e_) - 2) * 1024)
      auto wld = [&](auto S_) __attribute__((always_inline)) {
        constexpr int s = decltype(S_)::value == 11 ? 15 : decltype(S_)::value - 1;
        w_ld<3 * s>(wf[3 * s], wbo, wrs); w_ld<3 * s + 1>(wf[3 * s + 1], wbo, wrs); w_ld<3 * s + 2>(wf[3 * s + 2], wbo, wrs);
      };
      u32x4 gs[4], gx[4];
#define GO(e_) (((e_) & 1) * 1024 + ((e_) >> 1) * 8192)
#define G_BAD(h_) __any((NE2(gs[2 * (h_)][1], gs[2 * (h_)][3], stag) | NE2(gs[2 * (h_) + 1][1], gs[2 * (h_) + 1][3], stag) | NE2(gx[2 * (h_)][1], gx[2 * (h_)][3], mk) | NE2(gx[2 * (h_) + 1][1], gx[2 * (h_) + 1][3], mk)) != 0u)
      LAS char* const uw = (LAS char*)(size_t)opaque((u32)(L_U + (256 + r0) * 2));
      LAS char* const xw = (LAS char*)(size_t)opaque((u32)(L_X + r0 * 4));
      auto take = [&](auto H_) __attribute__((always_inline)) {
        constexpr int H = decltype(H_)::value;
        if (G_BAD(H)) {
          for (unsigned spins = 0;; ++spins) {
            FAILC(H ? 4 : 0);
            __builtin_amdgcn_s_sleep(SLP_U);
            asm volatile("" ::: "memory");
#pragma unroll
            for (int e = 2 * H; e < 2 * H + 2; ++e) { gs[e] = ldw(wrs, sto + GO(e)); gx[e] = ldw(wrs, GXO(e)); }
            if (!G_BAD(H) || spins > SPIN_CAP) break;
          }
        }
#pragma unroll
        for (int e = 2 * H; e < 2 * H + 2; ++e) {
          const float rs0 = __builtin_amdgcn_rcpf(hi2f(gs[e][0])), rs1 = __builtin_amdgcn_rcpf(hi2f(gs[e][2])), n0 = -lo2f(gs[e][0]) * rs0, n1 = -lo2f(gs[e][2]) * rs1;
          const int r = GO(e) / 8;
          *(LAS u32*)(uw + r * 2) = pkh(fmaf(lo2f(gx[e][0]), rs0, n0), fmaf(lo2f(gx[e][2]), rs1, n1));
          *(LAS u32*)(uw + 4608 + r * 2) = pkh(fmaf(hi2f(gx[e][0]), rs0, n0), fmaf(hi2f(gx[e][2]), rs1, n1));
          *(LAS u32x2*)(xw + r * 4) = (u32x2){gx[e][0], gx[e][2]};
        }
        __builtin_amdgcn_sched_barrier(0);
      };
      f32x4 (&cs)[2] = gs_.cs;
      u32x4 (&on)[3] = gs_.on;
      u32& ix = gs_.ix;
      auto wsum = [&](auto I_) __attribute__((always_inline)) { g_sum<decltype(I_)::value>(gs_, wf); };
      if (SLP_G) __builtin_amdgcn_s_sleep(SLP_G);
      asm volatile("" ::: "memory");
#define GLD(d_, r_, o_, so_) asm volatile("buffer_load_dwordx4 %0, %1, %2, %3 offen sc1" : "=&{" r_ "}"(d_) : "v"(o_), "s"(wrs), "s"(so_) : "memory")
      GLD(gs[0], "v[24:27]", sto, 0); GLD(gx[0], "v[28:31]", xlo, 0); GLD(gs[1], "v[32:35]", sto, GO(1)); GLD(gx[1], "v[36:39]", xlo, 16 * XBS);
      GLD(gs[2], "v[40:43]", sto, GO(2)); GLD(gx[2], "v[44:47]", xto, 0); GLD(gs[3], "v[48:51]", sto, GO(3)); GLD(gx[3], "v[52:55]", xto, 1024);
      ST(0, 5);
      {
        const u32 bm = (((g & 1) ? 0xFE0u : 0x1FF0u) << (ln & 15)) >> ((ln >> 4) * 8);
        u32x4 bw;
#pragma unroll
        for (int e = 0; e < 4; ++e) bw[e] = (((bm >> (2 * e)) & 1u) | (((bm >> (2 * e + 1)) & 1u) << 16)) * 0x3C00u;
        asm volatile("" : "+v"(bw));
        *(LAS u32x4*)(size_t)(u32)(L_BD + (g & 1) * 1024 + ln * 16) = bw;
        asm volatile("s_nop 1" :: "v"(bw) : "memory");
      }
      VMW(8 + 3 * (5 - R1E));
      __builtin_amdgcn_raw_buffer_store_b32(stag, wrs, fso, 0, 16);
      auto r2a = [&]() __attribute__((always_inline)) {
        cnt_spin(smem, C_A, 4);
        static_for<5, 7>(wraw);
      };
      auto pre = [&]() __attribute__((always_inline)) {
        const u32x4 SENT = {0xFFFFFFFFu, 0xFFFFFFFFu, 0xFFFFFFFFu, 0xFFFFFFFFu};
        const int l0 = 256 * g + 4 * ln;
        stw(wrs, OFF_X2 + (kp * XS_ + l0) * 4, SENT); stw(wrs, OFF_X2 + (kp * XS_ + l0 + 1024) * 4, SENT);
        stw(wrs, l0 < 32 ? OFF_ST2 + bid * 128 + ln * 16 : WSB, (u32x4){0, 0, 0, 0});
        __builtin_amdgcn_sched_barrier(0);
      };
      VMW(9);
      cnt_add(smem, C_W, ln);
      cnt_spin(smem, C_W, 4);
      ST(0, 4);
      __builtin_amdgcn_s_setprio(1);
      static_for<0, 5>(wget);
      asm volatile("s_waitcnt vmcnt(%4)" : "+{v[24:27]}"(gs[0]), "+{v[28:31]}"(gx[0]), "+{v[32:35]}"(gs[1]), "+{v[36:39]}"(gx[1]) : "n"(5) : "memory");
      take(IC<0>{});
      cnt_add(smem, C_U, ln);
      __builtin_amdgcn_s_setprio(0);
      __builtin_amdgcn_raw_buffer_store_b32(stag, wrs, ln == 0 ? OFF_ST0 + kp * 16 + g * 4 : WSB, 0, 16);
      asm volatile("s_waitcnt vmcnt(%4)" : "+{v[40:43]}"(gs[2]), "+{v[44:47]}"(gx[2]), "+{v[48:51]}"(gs[3]), "+{v[52:55]}"(gx[3]) : "n"(2) : "memory");
      ST(0, 3);
      pre();
      r2a();
      take(IC<1>{});
      cnt_add(smem, C_P2, ln);
      PAGE(9);
      VISIT(3); VISIT(10); VISIT(4);
      cnt_wait<1>(smem, C_S, 4);
      u32x4 fl;
      static_for<7, 7 + FLS>(wraw);
      if (SLP_FL) __builtin_amdgcn_s_sleep(SLP_FL);
      asm volatile("buffer_load_dwordx4 %0, %1, %2, 0 offen sc1" : "=&{v[56:59]}"(fl) : "v"(fo), "s"(wrs) : "memory");
      static_for<7 + FLS, 11>(wraw);
      ST(0, 7);
      {
        const u64 om[3] = {0x0030003000030003ull, 0x0300030000300030ull, 0x0003000303000300ull};
#pragma unroll
        for (int e = 0; e < 3; ++e) { const u32 v = __builtin_amdgcn_inverse_ballot_w64(om[e]) ? 0x3F803F80u : 0u; on[e] = (u32x4){v, v, v, v}; }
        ix = (ln & 1) ? 0xEEEEu : 0x4444u;
        cs[0] = cs[1] = (f32x4){0.f, 0.f, 0.f, 0.f};
        asm volatile("" : "+v"(on[0]), "+v"(on[1]), "+v"(on[2]), "+v"(ix), "+v"(cs[0]), "+v"(cs[1]));
      }
      asm volatile("s_waitcnt vmcnt(%1)" : "+{v[56:59]}"(fl) : "n"(3 * (4 - FLS)) : "memory");
      cnt_add(smem, C_W, ln);
      cnt_add(smem, C_P3, ln);
      ST(0, 17);
      tags_wait(fl, wrs, fo, stag, 15);
      static_for<11, HA>(wld);
      cnt_spin(smem, C_W, 8);
      static_for<5, 7>(wget);
      cnt_add(smem, C_G2, ln);
      ST(0, 6);
      static_for<0, CSA>(wsum);
      VMW(3 * (HA - 11));
      cnt_add(smem, C_W, ln);
      asm volatile("s_waitcnt lgkmcnt(1)" ::: "memory");
      static_for<CSA, 10>(wsum);
      cnt_spin(smem, C_W, 12);
      static_for<7, 11>(wget);
      ST(0, 9);
      g_pre(gp, g, true);
      const u32 wxo = opaque((u32)(L_WX + g * 3072 + ln * 16));
      VMW(3 * (HA - 13)); static_for<22, 24>(wsum); static_for<16, 17>(wsum);
#pragma unroll
      for (int e = 0; e < 3; ++e) *(LAS bf16x8*)(size_t)(wxo + e * 1024) = wf[45 + e];
      asm volatile("s_waitcnt lgkmcnt(3)" ::: "memory");
      static_for<10, 16>(wsum);
      cnt_wait<1>(smem, C_S, 8);
      static_for<HA, 16>(wld);
      ST(0, 8);
    }
    ConvPre pa2, pb2;
    float isc;
    u32 rp, rq;
    u32x4 xa, xb;
    g_blayer<true>(smem, wrs, g, stag, gp, gs_, wf);
    {
      const int ln2 = lane_id();
      const int l2 = 256 * g + 4 * ln2;
      const int xo2 = (int)opaque((u32)(OFF_X3 + (16 * g + (ln2 >> 2)) * XBS + kp * 64 + (ln2 & 1) * 32 + (ln2 & 2) * 8));
      isc = *(const float*)(smem + L_MI + 4 * (ln2 & 1));
      rp = (u32)(L_REDA + (ln2 & 1) * 8192 + (g * 64 + (ln2 ^ ((ln2 >> 3) & 3))) * 16); rq = rp ^ 16u;
      asm volatile("" : "+v"(isc), "+v"(rp), "+v"(rq));
      cnt_wait(smem, C_X2, 4);
      if (SLP_C2) __builtin_amdgcn_s_sleep(SLP_C2);
      u32x4 gg[4];
      ST(1, 2);
      x2_poll(wrs, kp, l2, xa);
      conv_unp(pa2, xa);
      if (SLP_S) __builtin_amdgcn_s_sleep(SLP_S);
      st2_poll(wrs, l2, gg);
      conv2_in(smem, l2, pa2, gg);
      ST(1, 3);
      lds_barrier();
      ST(1, 7);
      conv_xch(pa2);
      if (SLP_C3) __builtin_amdgcn_s_sleep(SLP_C3);
      xb = x2_ld(wrs, kp, l2 + 1024);
      st2_ld(wrs, l2 + 1024, gg);
      lds_barrier();
      ST(1, 8);
      f32x4 cq[4];
      conv_rd<0>(cq, rp, rq);
      stw(wrs, xo2, conv_epi(cq, isc, pa2));
      asm volatile("" : "+v"(xb), "+v"(gg[0]), "+v"(gg[1]), "+v"(gg[2]), "+v"(gg[3]) :: "memory");
      ST(1, 4);
      if (__any((umax4(0u, xb) == 0xFFFFFFFFu) | (st2_and(gg) == 0u))) {
        FAILC(18);
        xb = x2_ld(wrs, kp, l2 + 1024);
        st2_ld(wrs, l2 + 1024, gg);
        if (__any((umax4(0u, xb) == 0xFFFFFFFFu) | (st2_and(gg) == 0u))) {
          x2_poll(wrs, kp, l2 + 1024, xb);
          st2_poll(wrs, l2 + 1024, gg);
        }
        asm volatile("" : "+v"(xb), "+v"(gg[0]), "+v"(gg[1]), "+v"(gg[2]), "+v"(gg[3]));
      }
      ST(1, 1);
      conv_unp(pb2, xb);
      conv2_in(smem, l2 + 1024, pb2, gg);
      ST(1, 5);
      lds_barrier();
      ST(1, 6);
      const int rb = (bid & 7) * 4 + (bid >> 6);
      if (SLP_XA2) __builtin_amdgcn_s_sleep(SLP_XA2);
      x_dma(wrs, OFF_X3, 2 * rb, L_TA, g, ln2);
      PAGEV(13);
      conv_xch(pb2);
      g_pre(gp, g, false);
      XPre xp;
      x_pre(xp, 0, g, ln2, false);
      const int xo3 = (int)opaque((u32)(xo2 + 64 * XBS));
      lds_barrier();
      ST(1, 9);
      conv_rd<1024>(cq, rp, rq);
      auto epb = [&]() __attribute__((always_inline)) {
        asm volatile("" : "+v"(isc));
        stw(wrs, xo3, conv_epi(cq, isc, pb2));
        __builtin_amdgcn_sched_barrier(0);
      };
      { epb(); x_take<0, 1, false>(smem, wrs, OFF_X3, 2 * rb, g, ln2, xp, 0xFFFFFFFFu); }
    }
    g_blayer<false>(smem, wrs, g, stag, gp, gs_, wf);
    if ((bid | g) == 0) __builtin_amdgcn_raw_buffer_store_b32(stag, wrs, OFF_EP, 0, 16);
    asm volatile("s_barrier\n\ts_barrier" ::: "memory");
    ST_OUT();
  }
}

__device__ __attribute__((used, noinline)) void k_text_end() { asm volatile(".Lcw_end:"); }

extern "C" void kernel_launch(void* const* d_in, const int* in_sizes, int n_in, void* d_out, int out_size,
                              void* d_ws, size_t ws_size, hipStream_t stream) {
  (void)hipFuncSetAttribute((const void*)k_mega, hipFuncAttributeMaxDynamicSharedMemorySize, MEGA_LDS);
  k_mega<<<256, 512, MEGA_LDS, stream>>>((const u16*)d_in[0], (const u16*)d_in[1], (const u16*)d_in[2], (const u16*)d_in[3],
                                         (const u16*)d_in[4], (const u16*)d_in[5], (const u16*)d_in[6], (const u16*)d_in[7],
                                         (const u16*)d_in[8], (const u16*)d_in[10], (const u16*)d_in[11], (char*)d_ws,
                                         (u32*)d_out);
}
#if defined(__HIP_DEVICE_COMPILE__)
#pragma clang attribute pop
#endif
```
